```python
import torch

def _softmax(x, axis=-1):
    return torch.softmax(x, dim=axis)
def _gelu(x):
    return torch.nn.functional.gelu(x, approximate="tanh")
def _silu(x):
    return torch.nn.functional.silu(x)
def _sigmoid(x):
    return torch.sigmoid(x)
def _layernorm(x, axis=-1, eps=1e-5):
    m = x.mean(dim=axis, keepdim=True)
    v = x.var(dim=axis, keepdim=True, unbiased=False)
    return (x - m) / torch.sqrt(v + eps)
def _scatter_add(tgt, idx, src, axis=0):
    return tgt.clone().index_add_(axis, idx, src)
def _segment_sum(src, seg, n):
    out = torch.zeros((n,) + src.shape[1:], dtype=src.dtype)
    return out.index_add_(0, seg, src)
def _pad(x, pw):
    flat = tuple(v for pair in reversed(pw) for v in pair)
    return torch.nn.functional.pad(x, flat)

kernel_name = 'interleave_layers__mha_block__dense_activation_relu__prompt'

def setup_inputs():

    _w = (lambda a: (lambda t: t.to(torch.bfloat16) if t.is_floating_point() else t)(torch.as_tensor(a)))
    def _A_setup():
        B, S, D, H = 1, 2048, 512, 32
        return {
            "x": _w((lambda *s: torch.randn(*s, dtype=torch.bfloat16) * 0.05)(B, S, D)),
            "wqkv": _w((lambda *s: torch.randn(*s, dtype=torch.bfloat16) * 0.05)(D, 3 * D)),
            "wo": _w((lambda *s: torch.randn(*s, dtype=torch.bfloat16) * 0.05)(D, D)),
        }
    _A_in = _A_setup()

    def _B_setup():
        B, S, D, Dff = 1, 2048, 512, 1792
        return {
            "x": _w((lambda *s: torch.randn(*s, dtype=torch.bfloat16) * 0.05)(B, S, D)),
            "w1": _w((lambda *s: torch.randn(*s, dtype=torch.bfloat16) * 0.05)(D, Dff)),
            "w2": _w((lambda *s: torch.randn(*s, dtype=torch.bfloat16) * 0.05)(Dff, D)),
        }
    _B_in = _B_setup()

    return {
        "A_x": _A_in["x"],
        "A_wqkv": _A_in["wqkv"],
        "A_wo": _A_in["wo"],
        "B_x": _B_in["x"],
        "B_w1": _B_in["w1"],
        "B_w2": _B_in["w2"],
    }

def reference(A_x, A_wqkv, A_wo, B_x, B_w1, B_w2):
    def _A_ref(x, wqkv, wo):
        B, S, D = x.shape
        H = 32
        Dh = D // H
        qkv = (lambda x, *d: x.permute(*d))((x @ wqkv).reshape(B, S, 3, H, Dh), 2, 0, 3, 1, 4)
        q, k, v = qkv[0], qkv[1], qkv[2]
        scores = torch.einsum("bhqd,bhkd->bhqk", q, k) * (Dh ** -0.5)
        p = _softmax(scores, axis=-1)
        o = torch.einsum("bhqk,bhkd->bhqd", p, v)
        return (lambda x, *d: x.permute(*d))(o, 0, 2, 1, 3).reshape(B, S, D) @ wo

    def _B_ref(x, w1, w2):
        h = torch.einsum("bsd,df->bsf", x, w1)
        zero = torch.zeros_like(h)
        h = torch.maximum(h, zero)
        return torch.einsum("bsf,fd->bsd", h, w2)

    x = A_x
    x = x + _A_ref(_layernorm(x, axis=-1), A_wqkv, A_wo)
    x = x + _B_ref(_layernorm(x, axis=-1), B_w1, B_w2)
    x = x + _A_ref(_layernorm(x, axis=-1), A_wqkv, A_wo)
    x = x + _B_ref(_layernorm(x, axis=-1), B_w1, B_w2)
    return x
```

```cpp
#include <hip/hip_runtime.h>
#include <hip/hip_bf16.h>
#include <stdint.h>

namespace {

#define GAS __attribute__((address_space(1)))
#define LAS __attribute__((address_space(3)))
#define DI __device__ __forceinline__
typedef __bf16 bf16x8 __attribute__((ext_vector_type(8)));
typedef __bf16 bf16x2 __attribute__((ext_vector_type(2)));
typedef short s16x4 __attribute__((ext_vector_type(4)));
typedef float f32x2 __attribute__((ext_vector_type(2)));
typedef float f32x4 __attribute__((ext_vector_type(4)));
typedef float f32x16 __attribute__((ext_vector_type(16)));
typedef unsigned u32x2 __attribute__((ext_vector_type(2)));
typedef unsigned u32x4 __attribute__((ext_vector_type(4)));
typedef unsigned long long u64;
typedef __amdgpu_buffer_rsrc_t rsrc_t;

constexpr int NT = 512;
constexpr unsigned OFF_EPOCH = 0;
constexpr unsigned OFF_FLAGS = 0x1000;
constexpr unsigned OFF_PROF  = 0x2D00000;
constexpr unsigned OFF_CSQ   = 0x70000;
constexpr unsigned OFF_CS1   = 0x72000;
constexpr unsigned OFF_KM    = 0x74000;
constexpr unsigned OFF_ST    = 0x2C00000;
constexpr unsigned OFF_WQ    = 0x100000;
constexpr unsigned OFF_WO    = 0x280000;
constexpr unsigned OFF_W1    = 0x300000;
constexpr unsigned OFF_W2    = 0x4C0000;
constexpr unsigned OFF_XT    = 0x700000;
constexpr unsigned OFF_Q     = 0xA00000;
constexpr unsigned OFF_K     = 0xE00000;
constexpr unsigned OFF_V     = 0x1200000;
constexpr unsigned OFF_O     = 0x1600000;
constexpr unsigned OFF_X1    = 0x1A00000;
constexpr unsigned OFF_H     = 0x1E00000;
constexpr unsigned WS_BYTES  = 0x2E00000;
constexpr unsigned ACT_B = 0x200000, H_B = 0x700000, ST_B = 0x40000;
constexpr int PH_QKV = 1, PH_ATT = 2, PH_WO = 3, PH_F1 = 4, PH_F2 = 5, PH_PER_LAYER = 5;
constexpr int ATT_UNR = 5;
constexpr int LDS_K = 0, LDS_V = 65536, LDS_ONES = 131072, ONES_BYTES = (ATT_UNR + 2) * 1024 + 256;
constexpr int LDS_XCH = LDS_ONES + ONES_BYTES;
constexpr int LDS_FLAG = LDS_XCH + 8 * 2304;
constexpr int LDS_DUMMY = LDS_FLAG + 64;
constexpr int LDS_TOTAL = LDS_DUMMY + 256;
static_assert(LDS_TOTAL <= 163840, "LDS");

struct Params { const void* x; const void* wqkv; const void* wo; const void* w1; const void* w2; void* out; void* ws; };

#define MFMA(a, b, c) __builtin_amdgcn_mfma_f32_32x32x16_bf16((a), (b), (c), 0, 0, 0)
#define SBAR() __builtin_amdgcn_sched_barrier(0)
#define BAR() asm volatile("s_waitcnt lgkmcnt(0)\n\ts_barrier" ::: "memory")
#define WAITV_BAR(n) asm volatile("s_waitcnt vmcnt(%0) lgkmcnt(0)\n\ts_barrier" :: "i"(n) : "memory")
#define DRAIN() asm volatile("s_waitcnt vmcnt(0)" ::: "memory")

DI unsigned cvtpk(float lo, float hi) { f32x2 v = {lo, hi}; return __builtin_bit_cast(unsigned, __builtin_convertvector(v, bf16x2)); }
DI bf16x8 asbf(u32x4 v) { return __builtin_bit_cast(bf16x8, v); }
DI float bflo(unsigned u) { return __uint_as_float(u << 16); }
DI float bfhi(unsigned u) { return __uint_as_float(u & 0xffff0000u); }
DI rsrc_t mkrsrc(const void* p, unsigned bytes) { return __builtin_amdgcn_make_buffer_rsrc((void*)p, 0, bytes, 0x00020000); }
DI u32x4 ld16(rsrc_t rs, unsigned voff, unsigned soff) { return __builtin_amdgcn_raw_buffer_load_b128(rs, voff, soff, 16); }
DI unsigned ld4(rsrc_t rs, unsigned voff, unsigned soff) { return __builtin_amdgcn_raw_buffer_load_b32(rs, voff, soff, 16); }
DI void st16(rsrc_t rs, u32x4 v, unsigned voff, unsigned soff) { __builtin_amdgcn_raw_buffer_store_b128(v, rs, voff + soff, 0, 16); }
DI void st8(rsrc_t rs, u32x2 v, unsigned voff, unsigned soff) { __builtin_amdgcn_raw_buffer_store_b64(v, rs, voff, soff, 16); }
DI void st4(rsrc_t rs, unsigned v, unsigned voff, unsigned soff) { __builtin_amdgcn_raw_buffer_store_b32(v, rs, voff, soff, 16); }
#define DMA_HEAD "s_nop 2\n\ts_mov_b32 m0, %0\n\ts_nop 0\n\t"
#define DMA_ONE(off, pol) "buffer_load_dwordx4 %1, %2, %3 offen offset:" #off pol " lds\n\t"
template <int N> DI void dma(rsrc_t rs, unsigned voff, unsigned soff, unsigned ldsdst) {
    if constexpr (N == 1) asm volatile(DMA_HEAD DMA_ONE(0, " sc1") :: "s"(ldsdst), "v"(voff), "s"(rs), "s"(soff) : "memory");
    if constexpr (N == 2) asm volatile(DMA_HEAD DMA_ONE(0, " sc1") DMA_ONE(1024, " sc1") :: "s"(ldsdst), "v"(voff), "s"(rs), "s"(soff) : "memory");
    if constexpr (N == 3) asm volatile(DMA_HEAD DMA_ONE(0, " sc1") DMA_ONE(1024, " sc1") DMA_ONE(2048, " sc1") :: "s"(ldsdst), "v"(voff), "s"(rs), "s"(soff) : "memory");
    if constexpr (N == 4) asm volatile(DMA_HEAD DMA_ONE(0, " sc1") DMA_ONE(1024, " sc1") DMA_ONE(2048, " sc1") DMA_ONE(3072, " sc1") :: "s"(ldsdst), "v"(voff), "s"(rs), "s"(soff) : "memory");
}
DI void dma_in(rsrc_t rs, unsigned voff, unsigned soff, unsigned ldsdst) {
    asm volatile(DMA_HEAD DMA_ONE(0, "") :: "s"(ldsdst), "v"(voff), "s"(rs), "s"(soff) : "memory");
}
DI void warm4k(rsrc_t rs, unsigned l64, unsigned soff, unsigned ldsdst) {
    asm volatile("s_nop 2\n\ts_mov_b32 m0, %0\n\ts_nop 0\n\tbuffer_load_dword %1, %2, %3 offen lds" :: "s"(ldsdst), "v"(l64), "s"(rs), "s"(soff) : "memory");
}
#define OPAQUE_S(x) asm volatile("" : "+s"(x))
#define PIN4(a, i) asm volatile("" : "+v"(a[i]), "+v"(a[(i) + 1]), "+v"(a[(i) + 2]), "+v"(a[(i) + 3]))
DI u32x4 lds16(unsigned a) { return *(const LAS u32x4*)(uintptr_t)a; }
DI f32x4 ldsf4(unsigned a) { return *(const LAS f32x4*)(uintptr_t)a; }
DI s16x4 ldstr(unsigned a) { return __builtin_amdgcn_ds_read_tr16_b64_v4i16((LAS s16x4*)(uintptr_t)a); }
DI bf16x8 cat(s16x4 a, s16x4 b) { return __builtin_bit_cast(bf16x8, __builtin_shufflevector(a, b, 0, 1, 2, 3, 4, 5, 6, 7)); }
template <int CTRL> DI float dpp(float x) { return __builtin_bit_cast(float, __builtin_amdgcn_mov_dpp(__builtin_bit_cast(int, x), CTRL, 0xf, 0xf, true)); }
DI float swap32_sum(float x) { auto r = __builtin_amdgcn_permlane32_swap(__float_as_uint(x), __float_as_uint(x), false, false); return __uint_as_float(r[0]) + __uint_as_float(r[1]); }
DI float swap32_max(float x) { auto r = __builtin_amdgcn_permlane32_swap(__float_as_uint(x), __float_as_uint(x), false, false); return fmaxf(__uint_as_float(r[0]), __uint_as_float(r[1])); }
DI float swap32_lo(float x) { auto r = __builtin_amdgcn_permlane32_swap(__float_as_uint(x), __float_as_uint(x), false, false); return __uint_as_float(r[0]); }
DI float swap16_max(float x) { auto r = __builtin_amdgcn_permlane16_swap(__float_as_uint(x), __float_as_uint(x), false, false); return fmaxf(__uint_as_float(r[0]), __uint_as_float(r[1])); }
DI float swap16_sum(float x) { auto r = __builtin_amdgcn_permlane16_swap(__float_as_uint(x), __float_as_uint(x), false, false); return __uint_as_float(r[0]) + __uint_as_float(r[1]); }
DI float max32(float x) {
    x = fmaxf(x, dpp<0xB1>(x)); x = fmaxf(x, dpp<0x4E>(x)); x = fmaxf(x, dpp<0x141>(x)); x = fmaxf(x, dpp<0x140>(x)); return swap16_max(x); }
DI float sum8(float x) { x += dpp<0xB1>(x); x += dpp<0x4E>(x); x += dpp<0x141>(x); return x; }
DI float sum64(float x) { x = sum8(x); x += dpp<0x140>(x); x = swap16_sum(x); return swap32_sum(x); }

#define STAMP(i) do { } while (0)
#define WSTAMP(i) do { } while (0)
#define XST(i) do { } while (0)

struct Ctx {
    GAS char* ws; rsrc_t rs;
    u64 tag;
    unsigned lds0, l16;
    int lane, wid, b;
};
DI Ctx fresh(const Ctx& c0) {
    Ctx c = c0;
    int lane = __builtin_amdgcn_mbcnt_hi(~0u, __builtin_amdgcn_mbcnt_lo(~0u, 0u));
    asm volatile("" : "+v"(lane));
    c.lane = lane; c.l16 = lane * 16;
    asm volatile("" : "+s"(c.wid), "+s"(c.b), "+s"(c.lds0));
    return c;
}
DI int blk_chain(int s, int j) { return (s >> 2) + 8 * ((s & 3) * 8 + j); }
DI int blk_attn(int h, int g) { return (h & 7) + 8 * (4 * g + (h >> 3)); }

template <int N> DI void publish(const Ctx& c, int phase) {
    asm volatile("s_waitcnt vmcnt(%0) lgkmcnt(0)\n\ts_barrier" :: "i"(N) : "memory");
    if (c.wid == 0 && c.lane == 0) { u64 t = c.tag; asm volatile("" : "+s"(t)); t += (u64)phase; st8(c.rs, (u32x2){(unsigned)t, (unsigned)(t >> 32)}, 0, OFF_FLAGS + (phase * 256 + c.b) * 128); }
}
template <int PER_LANE> DI void await(const Ctx& c, int phase, int pb) {
    if (c.wid == 0) {
        u64 want = c.tag; asm volatile("" : "+s"(want)); want += (u64)phase;
        for (unsigned spins = 0; spins < (1u << 18); ++spins) {
            asm volatile("" ::: "memory");
            bool ok = true;
#pragma unroll
            for (int i = 0; i < PER_LANE; ++i) { const u32x2 f = __builtin_amdgcn_raw_buffer_load_b64(c.rs, pb * 128, OFF_FLAGS + (phase * 256 + 64 * i) * 128, 16);
                ok &= f[0] == (unsigned)want && f[1] == (unsigned)(want >> 32); }
            if (__all(ok)) break;
        }
    }
    BAR();
}

constexpr unsigned OOB = 0xF0000000u;

DI void qkv_store(const Ctx& c, const f32x16& acc, const f32x16& cs, float mu, float rstd, int kind, int pi, int l, int s, int j, int tb) {
    if (kind == 0) rstd *= 0.25f * 1.4426950408889634f;
    float z[16];
#pragma unroll
    for (int i = 0; i < 16; ++i) z[i] = (acc[i] - mu * cs[i]) * rstd;
    const int tbg = 2 * s + tb, hA = 4 * j + 2 * pi;
    const unsigned base = (kind == 0 ? OFF_Q : kind == 1 ? OFF_K : OFF_V) + l * ACT_B + (hA * 64 + tbg) * 1024;
    const unsigned vo = kind == 2 ? (c.lane & 31) * 32 + (c.lane >> 5) * 16 : c.l16;
    st16(c.rs, (u32x4){cvtpk(z[0], z[1]), cvtpk(z[2], z[3]), cvtpk(z[4], z[5]), cvtpk(z[6], z[7])}, vo, base);
    st16(c.rs, (u32x4){cvtpk(z[8], z[9]), cvtpk(z[10], z[11]), cvtpk(z[12], z[13]), cvtpk(z[14], z[15])}, vo, base + 64 * 1024);
    if (kind == 1) {
        float nA = 0.f, nB = 0.f;
#pragma unroll
        for (int i = 0; i < 8; ++i) { nA += z[i] * z[i]; nB += z[8 + i] * z[8 + i]; }
        nA = max32(swap32_sum(nA)); nB = max32(swap32_sum(nB));
        if (c.lane < 2) st4(c.rs, __float_as_uint(c.lane == 0 ? nA : nB), c.lane * 256, OFF_KM + ((l * 32 + hA) * 64 + tbg) * 4);
    }
}

DI void phase_first(Ctx& c0, const Params& p, int s, int j, unsigned (&xres)[8]) {
    Ctx c = fresh(c0); const int lane = c.lane, w = c.wid, b = c.b;
    const rsrc_t rx = mkrsrc(p.x, 2048 * 512 * 2), rq = mkrsrc(p.wqkv, 512 * 1536 * 2);
    {
        const unsigned vo = ((w >> 1) & 1) * 32768 + (lane & 31) * 1024 + (64 * j + 32 * (w & 1) + 4 * (lane >> 5)) * 2;
#pragma unroll
        for (int m = 0; m < 4; ++m) { const u32x2 t = __builtin_amdgcn_raw_buffer_load_b64(rx, vo + 16 * m, s * 65536, 0); xres[2 * m] = t[0]; xres[2 * m + 1] = t[1]; }
    }
    XST(97);
    rsrc_t rw = rq; unsigned ld = 1536, col0 = 0, ks0 = 4 * w, dst = OOB, csdst = OOB, src0 = OOB;
    if (b < 48) { const int jj = b / 6, i = b % 6; col0 = (i >> 1) * 512 + 64 * jj + 32 * (i & 1); dst = OFF_WQ + (b * 32 + ks0) * 1024; csdst = OFF_CSQ + b * 128; src0 = 0; }
    else if (b < 64) { rw = mkrsrc(p.wo, 512 * 512 * 2); ld = 512; col0 = 32 * (b - 48); dst = OFF_WO + ((b - 48) * 32 + ks0) * 1024; src0 = 0; }
    else if (b < 120) { rw = mkrsrc(p.w1, 512 * 1792 * 2); ld = 1792; col0 = 32 * (b - 64); dst = OFF_W1 + ((b - 64) * 32 + ks0) * 1024; csdst = OFF_CS1 + (b - 64) * 128; src0 = 0; }
    else if (b < 176) { rw = mkrsrc(p.w2, 1792 * 512 * 2); ld = 512; const int F = 32 * (b - 120) + 4 * w; col0 = 32 * (F / 112); ks0 = F % 112; dst = OFF_W2 + F * 1024; src0 = 0; }
    const unsigned stg = c.lds0 + 98304 + w * 4096;
    {
        const unsigned voff = ((lane >> 2) * ld + col0) * 2 + (lane & 3) * 16;
#pragma unroll
        for (int i = 0; i < 4; ++i) dma_in(rw, voff, src0 + (ks0 + i) * 16 * ld * 2, stg + i * 1024);
    }
    XST(98);
    const unsigned vX = (lane >> 3) * 1024 + (((lane & 7) ^ ((((8 * w + (lane >> 3)) & 31) >> 1) & 7)) * 16);
    const unsigned vW = (lane >> 3) * 3072 + (((lane & 7) ^ (((lane >> 4) & 1) << 2)) * 16);
#define FISSUE(ch) do { const unsigned sb_ = c.lds0 + ((ch) & 3) * 32768; \
        dma_in(rx, vX, (64 * s + 8 * w) * 1024 + (ch) * 128, sb_ + w * 1024); \
        _Pragma("unroll") for (int i = 0; i < 3; ++i) { const int pi_ = 3 * w + i; \
            dma_in(rq, vW, (64 * (ch) + 8 * (pi_ & 7)) * 3072 + ((pi_ >> 3) * 512 + 64 * j) * 2, sb_ + 8192 + pi_ * 1024); } } while (0)
    FISSUE(0); FISSUE(1); FISSUE(2);
    {
        u64 e; asm volatile("s_load_dwordx2 %0, %1, 0x0\n\ts_waitcnt lgkmcnt(0)" : "=s"(e) : "s"(p.ws) : "memory");
        c.tag = c0.tag = e + 1;
    }
    XST(86);
    {
        asm volatile("s_waitcnt vmcnt(12)" ::: "memory");
        XST(87);
        const int g = lane >> 4, h2 = g >> 1, nh = g & 1, q = (lane & 15) >> 2, pp = lane & 3;
        const unsigned ta = stg + (4 * h2 + q) * 64 + (16 * nh + 4 * pp) * 2;
        f32x16 cs = {};
        const bf16x8 ones = asbf((u32x4){0x3f803f80u, 0x3f803f80u, 0x3f803f80u, 0x3f803f80u});
#pragma unroll
        for (int i = 0; i < 4; ++i) {
            const bf16x8 f = cat(ldstr(ta + i * 1024), ldstr(ta + i * 1024 + 512));
            st16(c.rs, __builtin_bit_cast(u32x4, f), c.l16, dst + i * 1024);
            cs = MFMA(f, ones, cs);
        }
        if ((lane & 31) == 0) {
#pragma unroll
            for (int m = 0; m < 4; ++m) *(LAS f32x4*)(uintptr_t)(c.lds0 + LDS_XCH + 1024 + (w * 2 + (lane >> 5)) * 64 + m * 16) = (f32x4){cs[4 * m], cs[4 * m + 1], cs[4 * m + 2], cs[4 * m + 3]};
        }
    }
    const int nb = w < 4 ? w : 4 + (w & 1), r = lane & 31, h2 = lane >> 5;
    unsigned xa[4];
#pragma unroll
    for (int k = 0; k < 4; ++k) xa[k] = (r >> 3) * 1024 + (r & 7) * 128 + (((2 * k + h2) ^ ((r >> 1) & 7)) * 16) + (w < 4 ? 0 : ((w >> 1) & 1) * 4096);
    unsigned wa;
    { const int g = lane >> 4, q = (lane & 15) >> 2, pp = lane & 3;
      wa = 8192 + (nb >> 1) * 8192 + (8 * (g >> 1) + q) * 128 + ((4 * (nb & 1) + 2 * (g & 1) + (pp >> 1)) ^ ((q >> 1) << 2)) * 16 + (pp & 1) * 8; }
    const bf16x8 ones = asbf((u32x4){0x3f803f80u, 0x3f803f80u, 0x3f803f80u, 0x3f803f80u});
    f32x16 acc0 = {}, acc1 = {}, cs = {};
    float sm = 0.f, sq = 0.f;
#define FSTEP(ch) do { const unsigned sb_ = c.lds0 + ((ch) & 3) * 32768; \
        { const u32x4 xr = lds16(sb_ + w * 1024 + c.l16); \
          _Pragma("unroll") for (int i = 0; i < 4; ++i) { const float lo = bflo(xr[i]), hi = bfhi(xr[i]); sm += lo + hi; sq += lo * lo + hi * hi; } } \
        _Pragma("unroll") for (int k = 0; k < 4; ++k) { \
            const bf16x8 wfr = cat(ldstr(sb_ + wa + k * 2048), ldstr(sb_ + wa + k * 2048 + 512)); \
            acc0 = MFMA(wfr, asbf(lds16(sb_ + xa[k])), acc0); \
            if (w < 4) acc1 = MFMA(wfr, asbf(lds16(sb_ + xa[k] + 4096)), acc1); \
            cs = MFMA(wfr, ones, cs); } } while (0)
    XST(88);
    WAITV_BAR(12);
    XST(89);
    if (w == 0 && lane < 32) { float a = 0.f;
#pragma unroll
        for (int ww = 0; ww < 8; ++ww) a += *(const LAS float*)(uintptr_t)(c.lds0 + LDS_XCH + 1024 + ww * 128 + lane * 4);
        st4(c.rs, __float_as_uint(a), lane * 4, csdst); }
    FISSUE(3); FSTEP(0);
    WAITV_BAR(12); XST(90); FISSUE(4); FSTEP(1);
    WAITV_BAR(12); XST(91); FISSUE(5); FSTEP(2);
    WAITV_BAR(8);  XST(92); FISSUE(6); FSTEP(3);
    WAITV_BAR(8);  XST(93); FISSUE(7); FSTEP(4);
    WAITV_BAR(8);  XST(94); FSTEP(5);
    WAITV_BAR(4);  XST(95); FSTEP(6);
    WAITV_BAR(0);  XST(96); FSTEP(7);
#undef FISSUE
#undef FSTEP
    STAMP(4 * 1 + 2);
    sm = sum8(sm); sq = sum8(sq);
    if ((lane & 7) == 0) {
        const float mu = sm * (1.0f / 512.0f), var = fmaxf(sq * (1.0f / 512.0f) - mu * mu, 0.f);
        *(LAS f32x2*)(uintptr_t)(c.lds0 + LDS_XCH + (8 * w + (lane >> 3)) * 8) = (f32x2){mu, 1.0f / sqrtf(var + 1e-5f)};
    }
    BAR();
    if (w < 4) {
        const f32x2 t0 = *(const LAS f32x2*)(uintptr_t)(c.lds0 + LDS_XCH + r * 8), t1 = *(const LAS f32x2*)(uintptr_t)(c.lds0 + LDS_XCH + (32 + r) * 8);
        qkv_store(c, acc0, cs, t0[0], t0[1], w >> 1, w & 1, 0, s, j, 0);
        qkv_store(c, acc1, cs, t1[0], t1[1], w >> 1, w & 1, 0, s, j, 1);
    } else {
        const int tb = (w >> 1) & 1;
        const f32x2 t0 = *(const LAS f32x2*)(uintptr_t)(c.lds0 + LDS_XCH + (32 * tb + r) * 8);
        qkv_store(c, acc0, cs, t0[0], t0[1], 2, w & 1, 0, s, j, tb);
    }
    publish<0>(c, PH_QKV);
#pragma unroll
    for (int i = 0; i < 8; i += 4) PIN4(xres, i);
}

DI void issue_a512(const Ctx& c, int w, unsigned a_off  , unsigned st_off) {
    dma<1>(c.rs, c.l16, st_off + w * 1024, c.lds0 + LDS_XCH + w * 1024);
    const unsigned o = (w >> 2) * 32 + 2 * (w & 3);
#pragma unroll
    for (int ch = 0; ch < 4; ++ch) dma<2>(c.rs, c.l16, a_off + (o + 8 * ch) * 1024, c.lds0 + (o + 8 * ch) * 1024);
}
DI void row_stats(const Ctx& c, int tb, float& mu, float& rstd) {
    const unsigned a = c.lds0 + LDS_XCH + (32 * tb + (c.lane & 31)) * 128 + (c.lane >> 5) * 64;
    const f32x4 u0 = ldsf4(a), u1 = ldsf4(a + 16), u2 = ldsf4(a + 32), u3 = ldsf4(a + 48);
    const f32x4 u = (u0 + u1) + (u2 + u3);
    const float sm = swap32_sum(u[0] + u[2]), sq = swap32_sum(u[1] + u[3]);
    mu = sm * (1.0f / 512.0f);
    const float var = fmaxf(sq * (1.0f / 512.0f) - mu * mu, 0.f);
    rstd = 1.0f / sqrtf(var + 1e-5f);
}
#define KLOOP2(ab, wf, acc0, acc1, WAIT, AFTER) do { \
        u32x4 A0[3], A1[3]; \
        WAIT(0); \
        A0[0] = lds16((ab)); A1[0] = lds16((ab) + 32768); A0[1] = lds16((ab) + 1024); A1[1] = lds16((ab) + 33792); SBAR(); \
        _Pragma("unroll") for (int ks = 0; ks < 32; ++ks) { \
            if (ks == 6) { WAIT(1); } if (ks == 14) { WAIT(2); } if (ks == 22) { WAIT(3); } \
            if (ks + 2 < 32) { A0[(ks + 2) % 3] = lds16((ab) + (ks + 2) * 1024); A1[(ks + 2) % 3] = lds16((ab) + 32768 + (ks + 2) * 1024); } \
            SBAR(); \
            acc0 = MFMA(asbf(wf[ks]), asbf(A0[ks % 3]), acc0); acc1 = MFMA(asbf(wf[ks]), asbf(A1[ks % 3]), acc1); SBAR(); \
            AFTER(ks); SBAR(); \
        } } while (0)
#define KLOOP1(ab, wf, acc0, WAIT, AFTER) do { \
        u32x4 A0[4]; \
        WAIT(0); \
        A0[0] = lds16((ab)); A0[1] = lds16((ab) + 1024); A0[2] = lds16((ab) + 2048); SBAR(); \
        _Pragma("unroll") for (int ks = 0; ks < 32; ++ks) { \
            if (ks == 5) { WAIT(1); } if (ks == 13) { WAIT(2); } if (ks == 21) { WAIT(3); } \
            if (ks + 3 < 32) A0[(ks + 3) % 4] = lds16((ab) + (ks + 3) * 1024); \
            SBAR(); \
            acc0 = MFMA(asbf(wf[ks]), asbf(A0[ks % 4]), acc0); SBAR(); \
            AFTER(ks); SBAR(); \
        } } while (0)
DI void load_cs(const Ctx& c, unsigned off, f32x16& cs) {
#pragma unroll
    for (int m = 0; m < 4; ++m) { const f32x4 t = __builtin_bit_cast(f32x4, ld16(c.rs, (c.lane >> 5) * 64 + m * 16, off));
        cs[4 * m] = t[0]; cs[4 * m + 1] = t[1]; cs[4 * m + 2] = t[2]; cs[4 * m + 3] = t[3]; }
}

constexpr int QKV_PRE = 20;
DI unsigned wq_off(int w, int j) { return OFF_WQ + (6 * j + (w < 4 ? w : 4 + (w & 1))) * 32 * 1024; }
DI void phase_qkv1(const Ctx& c0, int s, int j, u32x4 (&wf)[32]) {
    const Ctx c = fresh(c0); const int w = c.wid; OPAQUE_S(s); OPAQUE_S(j);
    const int nb = w < 4 ? w : 4 + (w & 1);
    const unsigned wo_ = wq_off(w, j);
    f32x16 cs; load_cs(c, OFF_CSQ + (6 * j + nb) * 128, cs);
    await<1>(c, PH_F2, blk_chain(s, c.lane & 7));
    STAMP(4 * 6 + 1);
#pragma unroll
    for (int i = 0; i < QKV_PRE; i += 4) PIN4(wf, i);
    asm volatile("" : "+v"(cs));
    issue_a512(c, w, OFF_XT + 2 * s * 32 * 1024, OFF_ST + ST_B + s * 8192);
#pragma unroll
    for (int i = QKV_PRE; i < 32; ++i) wf[i] = ld16(c.rs, c.l16, wo_ + i * 1024);
    const unsigned ab = c.lds0 + c.l16;
#define QKV_WAIT(n) WAITV_BAR(6 - 2 * (n) + 32 - QKV_PRE)
    if (w < 4) {
        f32x16 acc0 = {}, acc1 = {}; float mu0, rs0, mu1, rs1;
#define QKV_AFTER(ks) do { if ((ks) == 24) row_stats(c, 0, mu0, rs0); if ((ks) == 27) row_stats(c, 1, mu1, rs1); } while (0)
        KLOOP2(ab, wf, acc0, acc1, QKV_WAIT, QKV_AFTER);
#undef QKV_AFTER
        STAMP(4 * 6 + 2);
        qkv_store(c, acc0, cs, mu0, rs0, w >> 1, w & 1, 1, s, j, 0);
        qkv_store(c, acc1, cs, mu1, rs1, w >> 1, w & 1, 1, s, j, 1);
    } else {
        const int tb = (w >> 1) & 1; const unsigned at = ab + tb * 32768;
        f32x16 acc0 = {}; float mu0, rs0;
#define QKV_AFTER(ks) do { if ((ks) == 24) row_stats(c, tb, mu0, rs0); } while (0)
        KLOOP1(at, wf, acc0, QKV_WAIT, QKV_AFTER);
#undef QKV_AFTER
        qkv_store(c, acc0, cs, mu0, rs0, 2, w & 1, 1, s, j, tb);
    }
#undef QKV_WAIT
    publish<0>(c, PH_QKV + PH_PER_LAYER);
}

#define W1PRE 8
#define W1MID 12
constexpr int W1_PRE = W1PRE, W1_MID = W1MID;
DI unsigned w1_off(int w, int j) { return w < 7 ? OFF_W1 + (7 * j + w) * 32 * 1024 : OOB; }
DI void phase_attn(const Ctx& c0, int l, int j, u32x4 (&wf)[32], u32x4 (&wo)[16]) {
    const Ctx c = fresh(c0); const int wid = c.wid, bb = c.b; OPAQUE_S(j);
    const int lane = c.lane, x = bb & 7, r = bb >> 3, h = x + 8 * (r & 3), g = r >> 2;
    const int qp = wid & 3, kh = wid >> 2;
    for (int i = wid * 64 + lane; i < ONES_BYTES / 4; i += NT) *(LAS unsigned*)(uintptr_t)(c.lds0 + LDS_ONES + i * 4) = (i & 7) == 0 ? 0x00003f80u : 0u;
    if (l == 0) await<4>(c, PH_QKV, lane);
    else await<1>(c, PH_QKV + PH_PER_LAYER, blk_chain(lane & 31, h >> 2));
    STAMP(4 * (2 + 5 * l) + 1);
    const unsigned kb = OFF_K + l * ACT_B + h * 65536, vb = OFF_V + l * ACT_B + h * 65536;
    u32x4 qrA, qrB; float km;
    {
        const unsigned qo = OFF_Q + l * ACT_B + (h * 64 + 8 * g + 2 * qp) * 1024, ko = OFF_KM + (l * 32 + h) * 256, l4 = lane * 4;
        asm volatile("s_nop 4\n\tbuffer_load_dwordx4 %0, %3, %4, %5 offen sc1\n\tbuffer_load_dwordx4 %1, %3, %4, %5 offen offset:1024 sc1\n\tbuffer_load_dword %2, %6, %4, %7 offen sc1"
                     : "=&v"(qrA), "=&v"(qrB), "=&v"(km) : "v"(c.l16), "s"(c.rs), "s"(qo), "v"(l4), "s"(ko) : "memory");
    }
    {
        const unsigned t0 = 32 * kh + 2 * qp, t1 = 32 * kh + 8 + 6 * qp;
        dma<2>(c.rs, c.l16, kb + t0 * 1024, c.lds0 + LDS_K + t0 * 1024);
        dma<2>(c.rs, c.l16, vb + t0 * 1024, c.lds0 + LDS_V + t0 * 1024);
        dma<4>(c.rs, c.l16, kb + t1 * 1024, c.lds0 + LDS_K + t1 * 1024);
        dma<4>(c.rs, c.l16, vb + t1 * 1024, c.lds0 + LDS_V + t1 * 1024);
        dma<2>(c.rs, c.l16, kb + (t1 + 4) * 1024, c.lds0 + LDS_K + (t1 + 4) * 1024);
        dma<2>(c.rs, c.l16, vb + (t1 + 4) * 1024, c.lds0 + LDS_V + (t1 + 4) * 1024);
    }
    asm volatile("s_waitcnt vmcnt(16)" : "+v"(qrA), "+v"(qrB), "+v"(km) :: "memory");
    km = swap32_max(max32(km));
    float q2A = 0.f, q2B = 0.f;
#pragma unroll
    for (int i = 0; i < 4; ++i) { float lo = bflo(qrA[i]), hi = bfhi(qrA[i]); q2A += lo * lo + hi * hi; lo = bflo(qrB[i]); hi = bfhi(qrB[i]); q2B += lo * lo + hi * hi; }
    q2A = swap32_sum(q2A); q2B = swap32_sum(q2B);
    float mA = __builtin_amdgcn_sqrtf(q2A * km) * 1.015625f, mB = __builtin_amdgcn_sqrtf(q2B * km) * 1.015625f;
    const bf16x8 qA = asbf(qrA), qB = asbf(qrB);
    const int grp = lane >> 4, h2 = grp >> 1, isone = grp & 1, qq = (lane & 15) >> 2, pp = lane & 3;
    const int inrow = qq * 32 + (pp & 1) * 16 + (pp >> 1) * 8;
    const int vstep = isone ? 0 : ATT_UNR * 1024, vstep2 = isone ? 0 : 2048;
    const unsigned o_off = OFF_O + l * ACT_B + ((8 * g + 2 * qp + kh) * 32 + h) * 1024;
    for (int pass = 0;; ++pass) {
        f32x16 nmA, nmB;
#pragma unroll
        for (int i = 0; i < 16; ++i) { nmA[i] = -mA; nmB[i] = -mB; }
        asm volatile("" : "+v"(nmA), "+v"(nmB));
        unsigned kpa = c.lds0 + LDS_K + kh * 32768 + c.l16; asm volatile("" : "+v"(kpa));
        unsigned vpa = c.lds0 + (isone ? LDS_ONES + (h2 ? 0 : 128) + inrow : LDS_V + kh * 32768 + h2 * 128 + inrow); asm volatile("" : "+v"(vpa));
        f32x16 oA = {}, oB = {};
#define KRD(t) asbf(lds16(kpa + (t) * 1024))
#define VTR(off) ldstr(vpa + (off))
        WAITV_BAR(12);
        bf16x8 kf = KRD(0);
        s16x4 va0 = VTR(0), va1 = VTR(256), vb0 = VTR(512), vb1 = VTR(768);
        f32x16 sA = MFMA(kf, qA, nmA), sB;
        u32x4 p0, p1;
#define EX(cc, i) do { cc[i] = __builtin_amdgcn_exp2f(cc[i]); SBAR(); } while (0)
#define CV(p, jj, cc, i) do { p[jj] = cvtpk(cc[i], cc[i + 1]); SBAR(); } while (0)
#define STREAM(cur, ocur, prv, oprv, qprv, nmprv, tl, ISA, PEND) do { \
        SBAR(); \
        EX(cur, 0); EX(cur, 1); if (PEND) CV(p1, 2, prv, 12); EX(cur, 2); if (PEND) CV(p1, 3, prv, 14); EX(cur, 3); EX(cur, 4); \
        if (PEND) { oprv = MFMA(cat(vb0, vb1), asbf(p1), oprv); SBAR(); } \
        if (ISA && PEND) { vb0 = VTR((tl) * 1024 + 512); vb1 = VTR((tl) * 1024 + 768); SBAR(); } \
        CV(p0, 0, cur, 0); EX(cur, 5); EX(cur, 6); CV(p0, 1, cur, 2); EX(cur, 7); EX(cur, 8); \
        prv = MFMA(kf, qprv, nmprv); SBAR(); \
        if (ISA) { kf = KRD((tl) + 1); SBAR(); } \
        CV(p0, 2, cur, 4); EX(cur, 9); EX(cur, 10); CV(p0, 3, cur, 6); EX(cur, 11); EX(cur, 12); \
        ocur = MFMA(cat(va0, va1), asbf(p0), ocur); SBAR(); \
        if (!(ISA)) { va0 = VTR(((tl) + 1) * 1024); va1 = VTR(((tl) + 1) * 1024 + 256); SBAR(); } \
        CV(p1, 0, cur, 8); EX(cur, 13); EX(cur, 14); CV(p1, 1, cur, 10); EX(cur, 15); \
    } while (0)
#define PAIR(tl, PEND) do { STREAM(sA, oA, sB, oB, qB, nmB, tl, true, PEND); STREAM(sB, oB, sA, oA, qA, nmA, tl, false, true); } while (0)
#define BUMP(n, vs) do { kpa += (n) * 1024; vpa += (vs); asm volatile("" : "+v"(kpa), "+v"(vpa)); } while (0)
        PAIR(0, false);
        PAIR(1, true);
        BUMP(2, vstep2);
#pragma unroll
        for (int u = 0; u < ATT_UNR; ++u) PAIR(u, true);
        BUMP(ATT_UNR, vstep);
        WAITV_BAR(0);
        STAMP(4 * (2 + 5 * l) + 0);
        if (l == 0) WSTAMP(64);
        {
            const unsigned o1 = OFF_WO + ((2 * j + (wid & 1)) * 32 + 16 * (wid >> 2)) * 1024, o2 = w1_off(wid, j);
#pragma unroll
            for (int i = 0; i < 16; ++i) wo[i] = ld16(c.rs, c.l16, o1 + i * 1024);
#pragma unroll
            for (int i = 0; i < W1_PRE; ++i) wf[i] = ld16(c.rs, c.l16, o2 + i * 1024);
        }
        for (int t = 2 + ATT_UNR; t < 32; t += ATT_UNR) {
#pragma unroll
            for (int u = 0; u < ATT_UNR; ++u) PAIR(u, true);
            BUMP(ATT_UNR, vstep);
        }
        CV(p1, 2, sB, 12); CV(p1, 3, sB, 14);
        oB = MFMA(cat(vb0, vb1), asbf(p1), oB);
        STAMP(4 * (2 + 5 * l) + 2);
        if (l == 0) WSTAMP(56);
        {
            const f32x16 give = kh ? oA : oB;
            const unsigned xb = c.lds0 + LDS_XCH + wid * 2304;
            *(LAS f32x4*)(uintptr_t)(xb + c.l16) = (f32x4){give[0], give[1], give[2], give[3]};
            *(LAS f32x4*)(uintptr_t)(xb + 1024 + c.l16) = (f32x4){give[4], give[5], give[6], give[7]};
            *(LAS float*)(uintptr_t)(xb + 2048 + lane * 4) = give[8];
            if (wid == 0 && lane == 0) *(LAS unsigned*)(uintptr_t)(c.lds0 + LDS_FLAG) = 0u;
        }
        BAR();
        f32x16 o = kh ? oB : oA;
        {
            const unsigned xb = c.lds0 + LDS_XCH + (wid ^ 4) * 2304;
            const f32x4 u0 = ldsf4(xb + c.l16), u1 = ldsf4(xb + 1024 + c.l16);
            const float ul = *(const LAS float*)(uintptr_t)(xb + 2048 + lane * 4);
#pragma unroll
            for (int i = 0; i < 4; ++i) { o[i] += u0[i]; o[4 + i] += u1[i]; }
            o[8] = swap32_lo(o[8] + ul);
        }
        const float rl = __builtin_amdgcn_rcpf(o[8]);
        st16(c.rs, (u32x4){cvtpk(o[0] * rl, o[1] * rl), cvtpk(o[2] * rl, o[3] * rl), cvtpk(o[4] * rl, o[5] * rl), cvtpk(o[6] * rl, o[7] * rl)}, c.l16, o_off);
        const bool bad = !(o[8] >= 8.673617379884035e-19f   && o[8] <= 3.0e38f);
        if (pass == 0 && __any(bad) && lane == 0) *(LAS unsigned*)(uintptr_t)(c.lds0 + LDS_FLAG) = 1u;
        WAITV_BAR(0);
        if (pass || *(const LAS unsigned*)(uintptr_t)(c.lds0 + LDS_FLAG) == 0u) break;
        mA = -3.0e38f; mB = -3.0e38f;
        const f32x16 zero = {};
        for (int t = 0; t < 64; ++t) {
            const bf16x8 kk = asbf(lds16(c.lds0 + LDS_K + t * 1024 + c.l16));
            const f32x16 a = MFMA(kk, qA, zero), bq = MFMA(kk, qB, zero);
#pragma unroll
            for (int i = 0; i < 16; ++i) { mA = fmaxf(mA, a[i]); mB = fmaxf(mB, bq[i]); }
        }
        mA = swap32_max(mA); mB = swap32_max(mB);
        BAR();
#undef KRD
#undef VTR
#undef EX
#undef CV
#undef STREAM
#undef PAIR
#undef BUMP
    }
    if (c.wid == 0 && c.lane == 0) { u64 t = c.tag; asm volatile("" : "+s"(t)); t += (u64)(PH_ATT + l * PH_PER_LAYER);
        st8(c.rs, (u32x2){(unsigned)t, (unsigned)(t >> 32)}, 0, OFF_FLAGS + ((PH_ATT + l * PH_PER_LAYER) * 256 + c.b) * 128); }
}

DI void res_finish(const Ctx& c, const Params& p, int w, int s, int j, bool last, const f32x16& v, unsigned x_off, unsigned st_off, unsigned (&xres)[8]) {
    const int lane = c.lane, tb = w >> 1, nb = w & 1;
#pragma unroll
    for (int m = 0; m < 8; ++m) xres[m] = cvtpk(bflo(xres[m]) + v[2 * m], bfhi(xres[m]) + v[2 * m + 1]);
    if (last) {
        const rsrc_t ro = mkrsrc(p.out, 2048 * 512 * 2);
        const unsigned vo = (64 * s + 32 * tb + (lane & 31)) * 1024 + (64 * j + 32 * nb) * 2 + (lane >> 5) * 16;
#pragma unroll
        for (int k = 0; k < 2; ++k) {
            const auto a = __builtin_amdgcn_permlane32_swap(xres[4 * k], xres[4 * k + 2], false, false);
            const auto b = __builtin_amdgcn_permlane32_swap(xres[4 * k + 1], xres[4 * k + 3], false, false);
            __builtin_amdgcn_raw_buffer_store_b128((u32x4){a[0], b[0], a[1], b[1]}, ro, vo + 32 * k, 0, 0);
        }
        return;
    }
    const unsigned base = x_off + ((2 * s + tb) * 32 + 4 * j + 2 * nb) * 1024;
    st16(c.rs, (u32x4){xres[0], xres[1], xres[2], xres[3]}, c.l16, base);
    st16(c.rs, (u32x4){xres[4], xres[5], xres[6], xres[7]}, c.l16, base + 1024);
    float sm = 0.f, sq = 0.f;
#pragma unroll
    for (int m = 0; m < 8; ++m) { const float lo = bflo(xres[m]), hi = bfhi(xres[m]); sm += lo + hi; sq += lo * lo + hi * hi; }
    sm = swap32_sum(sm); sq = swap32_sum(sq);
    if (lane < 32) st8(c.rs, (u32x2){__float_as_uint(sm), __float_as_uint(sq)}, lane * 128, st_off + (64 * s + 32 * tb) * 128 + (2 * j + nb) * 8);
}
DI void put16(unsigned a, const f32x16& v) {
#pragma unroll
    for (int m = 0; m < 4; ++m) *(LAS f32x4*)(uintptr_t)(a + m * 1024) = (f32x4){v[4 * m], v[4 * m + 1], v[4 * m + 2], v[4 * m + 3]};
}
DI void add16(unsigned a, f32x16& v) {
#pragma unroll
    for (int m = 0; m < 4; ++m) { const f32x4 t = ldsf4(a + m * 1024); v[4 * m] += t[0]; v[4 * m + 1] += t[1]; v[4 * m + 2] += t[2]; v[4 * m + 3] += t[3]; }
}

DI void phase_wo(const Ctx& c0, const Params& p, int l, int s, int j, unsigned (&xres)[8], u32x4 (&wo)[16], u32x4 (&wf)[32]) {
    const Ctx c = fresh(c0); const int w = c.wid; OPAQUE_S(s); OPAQUE_S(j);
    await<1>(c, PH_ATT + l * PH_PER_LAYER, blk_attn(c.lane & 31, s >> 2));
    STAMP(4 * (3 + 5 * l) + 1);
#pragma unroll
    for (int i = 0; i < 16; i += 4) PIN4(wo, i);
    {
        const unsigned o = (w & 1) * 32 + 16 * ((w >> 1) & 1) + 2 * (w >> 2), a_off = OFF_O + l * ACT_B + 2 * s * 32 * 1024;
#pragma unroll
        for (int ch = 0; ch < 4; ++ch) dma<2>(c.rs, c.l16, a_off + (o + 4 * ch) * 1024, c.lds0 + (o + 4 * ch) * 1024);
    }
    const unsigned nxt = w1_off(w, j);
#pragma unroll
    for (int i = W1_PRE; i < W1_PRE + W1_MID; ++i) wf[i] = ld16(c.rs, c.l16, nxt + i * 1024);
    const unsigned ab = c.lds0 + c.l16 + ((w >> 1) & 1) * 32768 + (w >> 2) * 16384;
    f32x16 acc = {};
#define WO_CH(ch) do { WAITV_BAR(6 - 2 * (ch) + W1_MID); u32x4 A[4]; \
        _Pragma("unroll") for (int i = 0; i < 4; ++i) A[i] = lds16(ab + (4 * (ch) + i) * 1024); SBAR(); \
        _Pragma("unroll") for (int i = 0; i < 4; ++i) { acc = MFMA(asbf(wo[4 * (ch) + i]), asbf(A[i]), acc); SBAR(); } } while (0)
    WO_CH(0); WO_CH(1); WO_CH(2); WO_CH(3);
#undef WO_CH
    STAMP(4 * (3 + 5 * l) + 2);
    const unsigned xa = c.lds0 + 65536 + (w & 3) * 4096 + c.l16;
    if (w >= 4) put16(xa, acc);
    BAR();
    if (w < 4) { add16(xa, acc); res_finish(c, p, w, s, j, false, acc, OFF_X1 + l * ACT_B, OFF_ST + (2 * l) * ST_B, xres); }
    asm volatile("" ::: "memory");
#pragma unroll
    for (int i = W1_PRE + W1_MID; i < 32; ++i) wf[i] = ld16(c.rs, c.l16, nxt + i * 1024);
    publish<32 - W1_PRE - W1_MID>(c, PH_WO + l * PH_PER_LAYER);
}

constexpr int W2_MID = 20;
DI unsigned w2_off(int w, int j) { return OFF_W2 + ((2 * j + (w & 1)) * 112 + 4 * (w >> 1)) * 1024; }
DI void phase_f1(const Ctx& c0, int l, int s, int j, u32x4 (&wf)[32]) {
    const Ctx c = fresh(c0); const int w = c.wid; OPAQUE_S(s); OPAQUE_S(j);
    const int nb = 7 * j + w;
    f32x16 cs; load_cs(c, w < 7 ? OFF_CS1 + nb * 128 : OOB, cs);
    await<1>(c, PH_WO + l * PH_PER_LAYER, blk_chain(s, c.lane & 7));
    STAMP(4 * (4 + 5 * l) + 1);
#pragma unroll
    for (int i = 0; i < 32; i += 4) PIN4(wf, i);
    asm volatile("" : "+v"(cs));
    if (l == 0) XST(79);
    issue_a512(c, w, OFF_X1 + l * ACT_B + 2 * s * 32 * 1024, OFF_ST + (2 * l) * ST_B + s * 8192);
    const unsigned ab = c.lds0 + c.l16, nxt = w2_off(w, j);
#define W2FRAG(i) ld16(c.rs, c.l16, nxt + (16 * ((i) >> 2) + ((i) & 3)) * 1024)
    f32x16 acc0 = {}, acc1 = {}; float mu0, rs0, mu1, rs1;
#define F1_WAIT(n) do { WAITV_BAR(6 - 2 * (n) + ((n) == 0 ? 0 : (n) == 1 ? 6 : (n) == 2 ? 14 : W2_MID)); if (l == 0) XST(80 + (n)); } while (0)
#define F1_AFTER(ks) do { if ((ks) < W2_MID) wf[ks] = W2FRAG(ks); if ((ks) == 24) row_stats(c, 0, mu0, rs0); if ((ks) == 27) row_stats(c, 1, mu1, rs1); } while (0)
    KLOOP2(ab, wf, acc0, acc1, F1_WAIT, F1_AFTER);
#undef F1_WAIT
#undef F1_AFTER
    STAMP(4 * (4 + 5 * l) + 2);
#pragma unroll
    for (int tb = 0; tb < 2; ++tb) {
        const float mu = tb ? mu1 : mu0, rstd = tb ? rs1 : rs0;
        const f32x16& acc = tb ? acc1 : acc0;
        float z[16];
#pragma unroll
        for (int i = 0; i < 16; ++i) z[i] = fmaxf((acc[i] - mu * cs[i]) * rstd, 0.f);
        const unsigned base = w < 7 ? OFF_H + l * H_B + ((2 * s + tb) * 112 + 2 * nb) * 1024 : OOB;
        st16(c.rs, (u32x4){cvtpk(z[0], z[1]), cvtpk(z[2], z[3]), cvtpk(z[4], z[5]), cvtpk(z[6], z[7])}, c.l16, base);
        st16(c.rs, (u32x4){cvtpk(z[8], z[9]), cvtpk(z[10], z[11]), cvtpk(z[12], z[13]), cvtpk(z[14], z[15])}, c.l16, base + 1024);
    }
    asm volatile("" ::: "memory");
    if (l == 0) XST(84);
#pragma unroll
    for (int i = W2_MID; i < 28; ++i) wf[i] = W2FRAG(i);
#undef W2FRAG
    publish<28 - W2_MID>(c, PH_F1 + l * PH_PER_LAYER);
}

DI void phase_f2(const Ctx& c0, const Params& p, int l, int s, int j, unsigned (&xres)[8], u32x4 (&wf)[32]) {
    const Ctx c = fresh(c0); const int w = c.wid; OPAQUE_S(s); OPAQUE_S(j);
    const int nb = w & 1, q = w >> 1, tbA = q == 1 ? 1 : 0;
    const bool last = l != 0;
    await<1>(c, PH_F1 + l * PH_PER_LAYER, blk_chain(s, c.lane & 7));
    STAMP(4 * (5 + 5 * l) + 1);
#pragma unroll
    for (int i = 0; i < 28; i += 4) PIN4(wf, i);
    const unsigned a_off = OFF_H + l * H_B + 2 * s * 112 * 1024;
#define ISSUE(ch) dma<4>(c.rs, c.l16, a_off + ((w >> 2) * 112 + 16 * (ch) + 4 * (w & 3)) * 1024, c.lds0 + ((ch) & 3) * 32768 + (w >> 2) * 16384 + 4 * (w & 3) * 1024)
    f32x16 acc0 = {}, acc1 = {};
    const unsigned ab0 = c.lds0 + c.l16 + q * 4096 + tbA * 16384, ab1 = c.lds0 + c.l16 + q * 4096 + (1 - tbA) * 16384;
#define COMPUTE(ch) do { const unsigned o_ = ((ch) & 3) * 32768; u32x4 A0[4], A1[4]; \
        _Pragma("unroll") for (int i = 0; i < 2; ++i) { A0[i] = lds16(ab0 + o_ + i * 1024); A1[i] = lds16(ab1 + o_ + i * 1024); } SBAR(); \
        _Pragma("unroll") for (int i = 0; i < 4; ++i) { \
            if (i < 2) { A0[i + 2] = lds16(ab0 + o_ + (i + 2) * 1024); A1[i + 2] = lds16(ab1 + o_ + (i + 2) * 1024); } SBAR(); \
            acc0 = MFMA(asbf(wf[4 * (ch) + i]), asbf(A0[i]), acc0); acc1 = MFMA(asbf(wf[4 * (ch) + i]), asbf(A1[i]), acc1); SBAR(); } } while (0)
    const unsigned nxt = last ? OOB : wq_off(w, j);
    constexpr int P = 8;
    ISSUE(0); ISSUE(1); ISSUE(2); ISSUE(3);
    WAITV_BAR(12); COMPUTE(0);
    WAITV_BAR(8); ISSUE(4); COMPUTE(1);
    WAITV_BAR(8); ISSUE(5); COMPUTE(2);
    WAITV_BAR(8); ISSUE(6); COMPUTE(3);
#pragma unroll
    for (int i = 0; i < P; ++i) wf[i] = ld16(c.rs, c.l16, nxt + i * 1024);
    WAITV_BAR(8 + P); COMPUTE(4);
    WAITV_BAR(4 + P); COMPUTE(5);
    WAITV_BAR(P); COMPUTE(6);
#undef ISSUE
#undef COMPUTE
    STAMP(4 * (5 + 5 * l) + 2);
    const int sl = q > 0 ? q - 1 : 0;
    put16(c.lds0 + ((nb + 2 * (1 - tbA)) * 3 + sl) * 4096 + c.l16, acc1);
    if (q >= 2) put16(c.lds0 + (nb * 3 + sl) * 4096 + c.l16, acc0);
    BAR();
    if (w < 4) {
        const unsigned xa = c.lds0 + w * 3 * 4096 + c.l16;
        add16(xa, acc0); add16(xa + 4096, acc0); add16(xa + 8192, acc0);
        res_finish(c, p, w, s, j, last, acc0, OFF_XT, OFF_ST + ST_B, xres);
    }
    if (last) return;
    asm volatile("" ::: "memory");
#pragma unroll
    for (int i = P; i < QKV_PRE; ++i) wf[i] = ld16(c.rs, c.l16, nxt + i * 1024);
    publish<QKV_PRE - P>(c, PH_F2);
}

__global__ void __launch_bounds__(NT, 2) fused_layers_kernel(Params p) {
    extern __shared__ __attribute__((aligned(16))) char lds[];
    Ctx c;
    c.ws = (GAS char*)p.ws; c.rs = mkrsrc(p.ws, WS_BYTES);
    c.lds0 = (unsigned)(uintptr_t)(LAS char*)lds;
    c.lane = threadIdx.x & 63; c.wid = __builtin_amdgcn_readfirstlane(threadIdx.x >> 6); c.l16 = c.lane * 16; c.b = blockIdx.x;
    c.tag = 0;
    const int x = c.b & 7, r = c.b >> 3, s = 4 * x + (r >> 3), j = r & 7;
    unsigned xres[8];
    u32x4 wf[32], wo[16];
    STAMP(0);
    phase_first(c, p, s, j, xres);
    STAMP(7);
#pragma unroll 1
    for (int l = 0;; ++l) {
        phase_attn(c, l, j, wf, wo);
        STAMP(4 * (2 + 5 * l) + 3);
        phase_wo(c, p, l, s, j, xres, wo, wf);
        STAMP(4 * (3 + 5 * l) + 3);
        phase_f1(c, l, s, j, wf);
        STAMP(4 * (4 + 5 * l) + 3);
        phase_f2(c, p, l, s, j, xres, wf);
        STAMP(4 * (5 + 5 * l) + 3);
        if (l != 0) break;
        phase_qkv1(c, s, j, wf);
        STAMP(4 * 6 + 3);
    }
    if (c.b == 0 && threadIdx.x == 0) __hip_atomic_store((GAS u64*)(c.ws + OFF_EPOCH), c.tag + 15, __ATOMIC_RELAXED, __HIP_MEMORY_SCOPE_AGENT);
}

}

extern "C" void kernel_launch(void* const* d_in, const int* in_sizes, int n_in, void* d_out, int out_size, void* d_ws, size_t ws_size, hipStream_t stream) {
    (void)in_sizes; (void)n_in; (void)out_size; (void)ws_size;
    Params p;
    p.x = d_in[0]; p.wqkv = d_in[1]; p.wo = d_in[2]; p.w1 = d_in[4]; p.w2 = d_in[5]; p.out = d_out; p.ws = d_ws;
    (void)hipFuncSetAttribute((const void*)fused_layers_kernel, hipFuncAttributeMaxDynamicSharedMemorySize, LDS_TOTAL);
    hipLaunchKernelGGL(fused_layers_kernel, dim3(256), dim3(NT), LDS_TOTAL, stream, p);
}
```

```cpp
#include <hip/hip_runtime.h>
#include <hip/hip_bf16.h>
#include <stdint.h>

namespace {

#define GAS __attribute__((address_space(1)))
#define LAS __attribute__((address_space(3)))
#define DI __device__ __forceinline__
typedef __bf16 bf16x8 __attribute__((ext_vector_type(8)));
typedef __bf16 bf16x2 __attribute__((ext_vector_type(2)));
typedef short s16x4 __attribute__((ext_vector_type(4)));
typedef float f32x2 __attribute__((ext_vector_type(2)));
typedef float f32x4 __attribute__((ext_vector_type(4)));
typedef float f32x16 __attribute__((ext_vector_type(16)));
typedef unsigned u32x2 __attribute__((ext_vector_type(2)));
typedef unsigned u32x4 __attribute__((ext_vector_type(4)));
typedef unsigned long long u64;
typedef __amdgpu_buffer_rsrc_t rsrc_t;

#define TRAIL 1
#define WARM 0
constexpr int NT = 512;
constexpr unsigned OFF_EPOCH = 0;
constexpr unsigned OFF_FLAGS = 0x1000;
constexpr unsigned OFF_PROF  = 0x2D00000;
constexpr unsigned OFF_CSQ   = 0x70000;
constexpr unsigned OFF_CS1   = 0x72000;
constexpr unsigned OFF_KM    = 0x74000;
constexpr unsigned OFF_ST    = 0x2C00000;
constexpr unsigned OFF_WQ    = 0x100000;
constexpr unsigned OFF_WO    = 0x280000;
constexpr unsigned OFF_W1    = 0x300000;
constexpr unsigned OFF_W2    = 0x4C0000;
constexpr unsigned OFF_XT    = 0x700000;
constexpr unsigned OFF_Q     = 0xA00000;
constexpr unsigned OFF_K     = 0xE00000;
constexpr unsigned OFF_V     = 0x1200000;
constexpr unsigned OFF_O     = 0x1600000;
constexpr unsigned OFF_X1    = 0x1A00000;
constexpr unsigned OFF_H     = 0x1E00000;
constexpr unsigned WS_BYTES  = 0x2E80000;
constexpr unsigned ACT_B = 0x200000, H_B = 0x700000, ST_B = 0x40000;
constexpr int PH_PREP = 0, PH_QKV = 1, PH_ATT = 2, PH_WO = 3, PH_F1 = 4, PH_F2 = 5, PH_PER_LAYER = 5;
constexpr int ATT_UNR = 7;
constexpr int LDS_K = 0, LDS_V = 65536, LDS_ONES = 131072, ONES_BYTES = (ATT_UNR + 2) * 1024 + 256;
constexpr int LDS_XCH = LDS_ONES + ONES_BYTES;
constexpr int LDS_FLAG = LDS_XCH + 8 * 2304;
constexpr int LDS_DUMMY = LDS_FLAG + 64;
constexpr int LDS_TOTAL = LDS_DUMMY + 4096;
static_assert(LDS_TOTAL <= 163840, "LDS");

struct Params { const void* x; const void* wqkv; const void* wo; const void* w1; const void* w2; void* out; void* ws; };

#define MFMA(a, b, c) __builtin_amdgcn_mfma_f32_32x32x16_bf16((a), (b), (c), 0, 0, 0)
#define SBAR() __builtin_amdgcn_sched_barrier(0)
#define BAR() asm volatile("s_waitcnt lgkmcnt(0)\n\ts_barrier" ::: "memory")
constexpr int waitcnt_imm(int vm, int lgkm) { return (vm & 15) | ((vm >> 4) << 14) | (7 << 4) | (lgkm << 8); }
#define WAITV_BAR(n) do { asm volatile("" ::: "memory"); __builtin_amdgcn_s_waitcnt(waitcnt_imm(n, 0)); asm volatile("s_barrier" ::: "memory"); } while (0)
#define ORDER() asm volatile("s_barrier" ::: "memory")
#define DRAIN() asm volatile("s_waitcnt vmcnt(0)" ::: "memory")

DI unsigned cvtpk(float lo, float hi) { f32x2 v = {lo, hi}; return __builtin_bit_cast(unsigned, __builtin_convertvector(v, bf16x2)); }
DI unsigned relupk(float lo, float hi) { typedef short s16x2 __attribute__((ext_vector_type(2)));
    const s16x2 v = __builtin_bit_cast(s16x2, cvtpk(lo, hi)); return __builtin_bit_cast(unsigned, __builtin_elementwise_max(v, (s16x2){0, 0})); }
DI bf16x8 asbf(u32x4 v) { return __builtin_bit_cast(bf16x8, v); }
DI float bflo(unsigned u) { return __uint_as_float(u << 16); }
DI float bfhi(unsigned u) { return __uint_as_float(u & 0xffff0000u); }
DI rsrc_t mkrsrc(const void* p, unsigned bytes) { return __builtin_amdgcn_make_buffer_rsrc((void*)p, 0, bytes, 0x00020000); }
DI u32x4 ld16(rsrc_t rs, unsigned voff, unsigned soff) { return __builtin_amdgcn_raw_buffer_load_b128(rs, voff, soff, 16); }
DI unsigned ld4(rsrc_t rs, unsigned voff, unsigned soff) { return __builtin_amdgcn_raw_buffer_load_b32(rs, voff, soff, 16); }
DI void st16(rsrc_t rs, u32x4 v, unsigned voff, unsigned soff) { __builtin_amdgcn_raw_buffer_store_b128(v, rs, voff + soff, 0, 16); }
DI void st8(rsrc_t rs, u32x2 v, unsigned voff, unsigned soff) { __builtin_amdgcn_raw_buffer_store_b64(v, rs, voff, soff, 16); }
DI void st4(rsrc_t rs, unsigned v, unsigned voff, unsigned soff) { __builtin_amdgcn_raw_buffer_store_b32(v, rs, voff, soff, 16); }
#define DMA_HEAD "s_nop 2\n\ts_mov_b32 m0, %0\n\ts_nop 0\n\t"
#define DMA_ONE(off, pol) "buffer_load_dwordx4 %1, %2, %3 offen offset:" #off pol " lds\n\t"
template <int N> DI void dma(rsrc_t rs, unsigned voff, unsigned soff, unsigned ldsdst) {
    if constexpr (N == 1) asm volatile(DMA_HEAD DMA_ONE(0, " sc1") :: "s"(ldsdst), "v"(voff), "s"(rs), "s"(soff) : "memory");
    if constexpr (N == 2) asm volatile(DMA_HEAD DMA_ONE(0, " sc1") DMA_ONE(1024, " sc1") :: "s"(ldsdst), "v"(voff), "s"(rs), "s"(soff) : "memory");
    if constexpr (N == 3) asm volatile(DMA_HEAD DMA_ONE(0, " sc1") DMA_ONE(1024, " sc1") DMA_ONE(2048, " sc1") :: "s"(ldsdst), "v"(voff), "s"(rs), "s"(soff) : "memory");
    if constexpr (N == 4) asm volatile(DMA_HEAD DMA_ONE(0, " sc1") DMA_ONE(1024, " sc1") DMA_ONE(2048, " sc1") DMA_ONE(3072, " sc1") :: "s"(ldsdst), "v"(voff), "s"(rs), "s"(soff) : "memory");
}
DI void dma_in(rsrc_t rs, unsigned voff, unsigned soff, unsigned ldsdst) {
    asm volatile(DMA_HEAD DMA_ONE(0, "") :: "s"(ldsdst), "v"(voff), "s"(rs), "s"(soff) : "memory");
}
DI void warm4k(rsrc_t rs, unsigned l64, unsigned soff, unsigned ldsdst) {
    asm volatile("s_nop 2\n\ts_mov_b32 m0, %0\n\ts_nop 0\n\tbuffer_load_dword %1, %2, %3 offen lds" :: "s"(ldsdst), "v"(l64), "s"(rs), "s"(soff) : "memory");
}
#define OPAQUE_S(x) asm volatile("" : "+s"(x))
#define PIN4(a, i) asm volatile("" : "+v"(a[i]), "+v"(a[(i) + 1]), "+v"(a[(i) + 2]), "+v"(a[(i) + 3]))
DI u32x4 lds16(unsigned a) { return *(const LAS u32x4*)(uintptr_t)a; }
DI f32x4 ldsf4(unsigned a) { return *(const LAS f32x4*)(uintptr_t)a; }
DI s16x4 ldstr(unsigned a) { return __builtin_amdgcn_ds_read_tr16_b64_v4i16((LAS s16x4*)(uintptr_t)a); }
DI bf16x8 cat(s16x4 a, s16x4 b) { return __builtin_bit_cast(bf16x8, __builtin_shufflevector(a, b, 0, 1, 2, 3, 4, 5, 6, 7)); }
template <int CTRL> DI float dpp(float x) { return __builtin_bit_cast(float, __builtin_amdgcn_mov_dpp(__builtin_bit_cast(int, x), CTRL, 0xf, 0xf, true)); }
DI float swap32_sum(float x) { auto r = __builtin_amdgcn_permlane32_swap(__float_as_uint(x), __float_as_uint(x), false, false); return __uint_as_float(r[0]) + __uint_as_float(r[1]); }
DI float swap32_max(float x) { auto r = __builtin_amdgcn_permlane32_swap(__float_as_uint(x), __float_as_uint(x), false, false); return fmaxf(__uint_as_float(r[0]), __uint_as_float(r[1])); }
DI float swap32_lo(float x) { auto r = __builtin_amdgcn_permlane32_swap(__float_as_uint(x), __float_as_uint(x), false, false); return __uint_as_float(r[0]); }
DI float swap16_max(float x) { auto r = __builtin_amdgcn_permlane16_swap(__float_as_uint(x), __float_as_uint(x), false, false); return fmaxf(__uint_as_float(r[0]), __uint_as_float(r[1])); }
DI float swap16_sum(float x) { auto r = __builtin_amdgcn_permlane16_swap(__float_as_uint(x), __float_as_uint(x), false, false); return __uint_as_float(r[0]) + __uint_as_float(r[1]); }
DI float max32(float x) {
    x = fmaxf(x, dpp<0xB1>(x)); x = fmaxf(x, dpp<0x4E>(x)); x = fmaxf(x, dpp<0x141>(x)); x = fmaxf(x, dpp<0x140>(x)); return swap16_max(x); }
DI float sum8(float x) { x += dpp<0xB1>(x); x += dpp<0x4E>(x); x += dpp<0x141>(x); return x; }
DI float sum64(float x) { x = sum8(x); x += dpp<0x140>(x); x = swap16_sum(x); return swap32_sum(x); }

#define STAMP(i) do { } while (0)
#define ESTAMP(i) do { } while (0)
#define WSTAMP(i) do { } while (0)
#define XST(i) do { } while (0)

#define TSDECL do { } while (0)
#define TS(i) do { } while (0)
#define TSFLUSH(base) do { } while (0)
struct Ctx {
    GAS char* ws; rsrc_t rs;
    u64 tag;
    unsigned lds0, l16;
    int lane, wid, b;
};
DI Ctx fresh(const Ctx& c0) {
    Ctx c = c0;
    int lane = __builtin_amdgcn_mbcnt_hi(~0u, __builtin_amdgcn_mbcnt_lo(~0u, 0u));
    asm volatile("" : "+v"(lane));
    c.lane = lane; c.l16 = lane * 16;
    asm volatile("" : "+s"(c.wid), "+s"(c.b), "+s"(c.lds0));
    return c;
}
DI int blk_chain(int s, int j) { return (s >> 2) + 8 * ((s & 3) * 8 + j); }
DI int blk_attn(int h, int g) { return (h & 7) + 8 * (4 * g + (h >> 3)); }
#define ROT 0
DI int rot32(int s, int j) { return ROT ? (4 * j + 8 * (s & 3)) & 31 : 0; }
DI int rot7(int s, int j) { const int t = j + 2 * (s & 3); return ROT ? (t >= 7 ? (t >= 14 ? t - 14 : t - 7) : t) : 0; }
DI int chunk7(int n, int r) { const int t = n + r; return t >= 7 ? t - 7 : t; }
DI unsigned fr32(unsigned base, int n, int rot) { return base + ((n + rot) & 31) * 1024; }

template <int N> DI void publish(const Ctx& c, int phase) {
    WAITV_BAR(N);
    if (c.wid == 0 && c.lane == 0) { u64 t = c.tag; asm volatile("" : "+s"(t)); t += (u64)phase; st8(c.rs, (u32x2){(unsigned)t, (unsigned)(t >> 32)}, 0, OFF_FLAGS + (phase * 256 + c.b) * 128); }
}
template <int PER_LANE> DI void await(const Ctx& c, int phase, int pb) {
    if (c.wid == 0) {
        u64 want = c.tag; asm volatile("" : "+s"(want)); want += (u64)phase;
        for (unsigned spins = 0; spins < (1u << 18); ++spins) {
            asm volatile("" ::: "memory");
            bool ok = true;
#pragma unroll
            for (int i = 0; i < PER_LANE; ++i) { const u32x2 f = __builtin_amdgcn_raw_buffer_load_b64(c.rs, pb * 128, OFF_FLAGS + (phase * 256 + 64 * i) * 128, 16);
                ok &= f[0] == (unsigned)want && f[1] == (unsigned)(want >> 32); }
            if (__all(ok)) break;
        }
    }
    BAR();
}

constexpr unsigned OOB = 0xF0000000u;

DI void qkv_store(const Ctx& c, const f32x16& acc, const f32x16& cs, float mu, float rstd, int kind, int pi, int l, int s, int j, int tb) {
    if (kind == 0) rstd *= 0.25f * 1.4426950408889634f;
    float z[16];
#pragma unroll
    for (int i = 0; i < 16; ++i) z[i] = (acc[i] - mu * cs[i]) * rstd;
    const int tbg = 2 * s + tb, hA = 4 * j + 2 * pi;
    const unsigned base = (kind == 0 ? OFF_Q : kind == 1 ? OFF_K : OFF_V) + l * ACT_B + (hA * 64 + tbg) * 1024;
    const unsigned vo = kind == 2 ? (c.lane & 31) * 32 + (c.lane >> 5) * 16 : c.l16;
    st16(c.rs, (u32x4){cvtpk(z[0], z[1]), cvtpk(z[2], z[3]), cvtpk(z[4], z[5]), cvtpk(z[6], z[7])}, vo, base);
    st16(c.rs, (u32x4){cvtpk(z[8], z[9]), cvtpk(z[10], z[11]), cvtpk(z[12], z[13]), cvtpk(z[14], z[15])}, vo, base + 64 * 1024);
    if (kind == 1) {
        float nA = 0.f, nB = 0.f;
#pragma unroll
        for (int i = 0; i < 8; ++i) { nA += z[i] * z[i]; nB += z[8 + i] * z[8 + i]; }
        nA = max32(swap32_sum(nA)); nB = max32(swap32_sum(nB));
        if (c.lane < 2) st4(c.rs, __float_as_uint(c.lane == 0 ? nA : nB), c.lane * 256, OFF_KM + ((l * 32 + hA) * 64 + tbg) * 4);
    }
}

DI void phase_first(Ctx& c0, const Params& p, int s, int j) {
    Ctx c = fresh(c0); const int lane = c.lane, w = c.wid, b = c.b;
    TSDECL; TS(0);
    const rsrc_t rx = mkrsrc(p.x, 2048 * 512 * 2), rq = mkrsrc(p.wqkv, 512 * 1536 * 2);
    const unsigned vX = (lane >> 3) * 1024 + (((lane & 7) ^ ((((8 * w + (lane >> 3)) & 31) >> 1) & 7)) * 16);
    const unsigned vW = (lane >> 3) * 3072 + (((lane & 7) ^ (((lane >> 4) & 1) << 2)) * 16);
    const int rot8 = ROT ? (j + 2 * (s & 3)) & 7 : 0;
#define FISSUE(ch) do { const unsigned sb_ = c.lds0 + ((ch) & 3) * 32768; const int ca_ = ((ch) + rot8) & 7; \
        dma_in(rx, vX, (64 * s + 8 * w) * 1024 + ca_ * 128, sb_ + w * 1024); \
        _Pragma("unroll") for (int i = 0; i < 3; ++i) { const int pi_ = 3 * w + i; \
            dma_in(rq, vW, (64 * ca_ + 8 * (pi_ & 7)) * 3072 + ((pi_ >> 3) * 512 + 64 * j) * 2, sb_ + 8192 + pi_ * 1024); } } while (0)
    FISSUE(0); ORDER(); FISSUE(1); FISSUE(2); FISSUE(3);
    {
        u64 e; asm volatile("s_load_dwordx2 %0, %1, 0x0\n\ts_waitcnt lgkmcnt(0)" : "=s"(e) : "s"(p.ws) : "memory");
        c.tag = c0.tag = e + 1;
    }
    XST(86); TS(1);
    const int nb = w < 4 ? w : 4 + (w & 1), r = lane & 31, h2 = lane >> 5;
    unsigned xa[4];
#pragma unroll
    for (int k = 0; k < 4; ++k) xa[k] = (r >> 3) * 1024 + (r & 7) * 128 + (((2 * k + h2) ^ ((r >> 1) & 7)) * 16) + (w < 4 ? 0 : ((w >> 1) & 1) * 4096);
    unsigned wa;
    { const int g = lane >> 4, q = (lane & 15) >> 2, pp = lane & 3;
      wa = 8192 + (nb >> 1) * 8192 + (8 * (g >> 1) + q) * 128 + ((4 * (nb & 1) + 2 * (g & 1) + (pp >> 1)) ^ ((q >> 1) << 2)) * 16 + (pp & 1) * 8; }
    const bf16x8 ones = asbf((u32x4){0x3f803f80u, 0x3f803f80u, 0x3f803f80u, 0x3f803f80u});
    f32x16 acc0 = {}, acc1 = {}, cs = {};
    float sm = 0.f, sq = 0.f;
#define FSTEP(ch) do { const unsigned sb_ = c.lds0 + ((ch) & 3) * 32768; \
        const u32x4 xr = lds16(sb_ + w * 1024 + c.l16); \
        bf16x8 wfr[4]; u32x4 x0[4], x1[4]; \
        _Pragma("unroll") for (int k = 0; k < 4; ++k) { wfr[k] = cat(ldstr(sb_ + wa + k * 2048), ldstr(sb_ + wa + k * 2048 + 512)); x0[k] = lds16(sb_ + xa[k]); if (w < 4) x1[k] = lds16(sb_ + xa[k] + 4096); } \
        _Pragma("unroll") for (int i = 0; i < 4; ++i) { const float lo = bflo(xr[i]), hi = bfhi(xr[i]); sm += lo + hi; sq += lo * lo + hi * hi; } \
        _Pragma("unroll") for (int k = 0; k < 4; ++k) { \
            acc0 = MFMA(wfr[k], asbf(x0[k]), acc0); \
            if (w < 4) acc1 = MFMA(wfr[k], asbf(x1[k]), acc1); \
            cs = MFMA(wfr[k], ones, cs); } } while (0)
    rsrc_t rw = rq; unsigned ld = 1536, col0 = 0, ks0 = 4 * w, dst = OOB, csdst = OOB, src0 = OOB;
    if (b < 48) { const int jj = b / 6, i = b % 6; col0 = (i >> 1) * 512 + 64 * jj + 32 * (i & 1); dst = OFF_WQ + (b * 32 + ks0) * 1024; csdst = OFF_CSQ + b * 128; src0 = 0; }
    else if (b < 64) { rw = mkrsrc(p.wo, 512 * 512 * 2); ld = 512; col0 = 32 * (b - 48); dst = OFF_WO + ((b - 48) * 32 + ks0) * 1024; src0 = 0; }
    else if (b < 120) { rw = mkrsrc(p.w1, 512 * 1792 * 2); ld = 1792; col0 = 32 * (b - 64); dst = OFF_W1 + ((b - 64) * 32 + ks0) * 1024; csdst = OFF_CS1 + (b - 64) * 128; src0 = 0; }
    else if (b < 176) { rw = mkrsrc(p.w2, 1792 * 512 * 2); ld = 512; const int F = 32 * (b - 120) + 4 * w; col0 = 32 * (F / 112); ks0 = F % 112; dst = OFF_W2 + F * 1024; src0 = 0; }
    const unsigned stg = c.lds0 + w * 4096;
    WAITV_BAR(12); XST(89); TS(2); FSTEP(0);
    WAITV_BAR(8);  XST(90); FISSUE(4); FSTEP(1);
    WAITV_BAR(8);  XST(91); FISSUE(5); FSTEP(2);
    WAITV_BAR(8);  XST(92); FISSUE(6); FSTEP(3);
    WAITV_BAR(8);  XST(93); TS(3); FISSUE(7); FSTEP(4);
    WAITV_BAR(8);  XST(94);
    {
        const unsigned voff = ((lane >> 2) * ld + col0) * 2 + (lane & 3) * 16;
#pragma unroll
        for (int i = 0; i < 4; ++i) dma_in(rw, voff, src0 + (ks0 + i) * 16 * ld * 2, stg + i * 1024);
    }
    FSTEP(5);
    WAITV_BAR(8);  XST(95); FSTEP(6);
    WAITV_BAR(4);  XST(96); TS(4); FSTEP(7);
#undef FISSUE
#undef FSTEP
    STAMP(4 * 1 + 2); TS(5);
    sm = sum8(sm); sq = sum8(sq);
    if ((lane & 7) == 0) {
        const float mu = sm * (1.0f / 512.0f), var = fmaxf(sq * (1.0f / 512.0f) - mu * mu, 0.f);
        *(LAS f32x2*)(uintptr_t)(c.lds0 + LDS_XCH + (8 * w + (lane >> 3)) * 8) = (f32x2){mu, __builtin_amdgcn_rsqf(var + 1e-5f)};
    }
    BAR();
    if (w < 4) {
        const f32x2 t0 = *(const LAS f32x2*)(uintptr_t)(c.lds0 + LDS_XCH + r * 8), t1 = *(const LAS f32x2*)(uintptr_t)(c.lds0 + LDS_XCH + (32 + r) * 8);
        qkv_store(c, acc0, cs, t0[0], t0[1], w >> 1, w & 1, 0, s, j, 0);
        qkv_store(c, acc1, cs, t1[0], t1[1], w >> 1, w & 1, 0, s, j, 1);
    } else {
        const int tb = (w >> 1) & 1;
        const f32x2 t0 = *(const LAS f32x2*)(uintptr_t)(c.lds0 + LDS_XCH + (32 * tb + r) * 8);
        qkv_store(c, acc0, cs, t0[0], t0[1], 2, w & 1, 0, s, j, tb);
    }
    TS(6);
    publish<0>(c, PH_QKV);
    TS(7); TSFLUSH(0);
    {
        const int g = lane >> 4, hh = g >> 1, nh = g & 1, q = (lane & 15) >> 2, pp = lane & 3;
        const unsigned ta = stg + (4 * hh + q) * 64 + (16 * nh + 4 * pp) * 2;
        f32x16 cw = {};
#pragma unroll
        for (int i = 0; i < 4; ++i) {
            const bf16x8 f = cat(ldstr(ta + i * 1024), ldstr(ta + i * 1024 + 512));
            st16(c.rs, __builtin_bit_cast(u32x4, f), c.l16, dst + i * 1024);
            cw = MFMA(f, ones, cw);
        }
        if ((lane & 31) == 0) {
#pragma unroll
            for (int m = 0; m < 4; ++m) *(LAS f32x4*)(uintptr_t)(c.lds0 + LDS_XCH + 1024 + (w * 2 + (lane >> 5)) * 64 + m * 16) = (f32x4){cw[4 * m], cw[4 * m + 1], cw[4 * m + 2], cw[4 * m + 3]};
        }
        BAR();
        if (w == 0 && lane < 32) { float a = 0.f;
#pragma unroll
            for (int ww = 0; ww < 8; ++ww) a += *(const LAS float*)(uintptr_t)(c.lds0 + LDS_XCH + 1024 + ww * 128 + lane * 4);
            st4(c.rs, __float_as_uint(a), lane * 4, csdst); }
        publish<0>(c, PH_PREP);
    }
}

DI void issue_a512(const Ctx& c, int w, unsigned a_off  , unsigned st_off, int rot) {
    const unsigned tbo = (w >> 2) * 32768, n0 = 2 * (w & 3);
    dma<1>(c.rs, c.l16, st_off + w * 1024, c.lds0 + LDS_XCH + w * 1024);
    dma<2>(c.rs, c.l16, fr32(a_off + tbo, n0, rot), c.lds0 + tbo + n0 * 1024);
    ORDER();
#pragma unroll
    for (int ch = 1; ch < 4; ++ch) dma<2>(c.rs, c.l16, fr32(a_off + tbo, n0 + 8 * ch, rot), c.lds0 + tbo + (n0 + 8 * ch) * 1024);
    ORDER();
}
struct RowStat { f32x4 u0, u1, u2, u3; };
DI void stat_read(const Ctx& c, int tb, RowStat& t) {
    const unsigned r = c.lane & 31, a = c.lds0 + LDS_XCH + (32 * tb + r) * 128, k = ((4 * (c.lane >> 5)) ^ ((r >> 1) & 7)) * 16;
    t.u0 = ldsf4(a + k); t.u1 = ldsf4(a + (k ^ 16)); t.u2 = ldsf4(a + (k ^ 32)); t.u3 = ldsf4(a + (k ^ 48));
}
DI void stat_finish(const RowStat& t, float& mu, float& rstd) {
    const float sm = swap32_sum(((t.u0[0] + t.u0[2]) + (t.u1[0] + t.u1[2])) + ((t.u2[0] + t.u2[2]) + (t.u3[0] + t.u3[2])));
    const float sq = swap32_sum(((t.u0[1] + t.u0[3]) + (t.u1[1] + t.u1[3])) + ((t.u2[1] + t.u2[3]) + (t.u3[1] + t.u3[3])));
    mu = sm * (1.0f / 512.0f);
    rstd = __builtin_amdgcn_rsqf(fmaxf(sq * (1.0f / 512.0f) - mu * mu, 0.f) + 1e-5f);
}
#define KLOOP2(ab, wf, acc0, acc1, WAIT, AFTER) do { \
        u32x4 A0[3], A1[3]; \
        WAIT(0); \
        A0[0] = lds16((ab)); A1[0] = lds16((ab) + 32768); A0[1] = lds16((ab) + 1024); A1[1] = lds16((ab) + 33792); SBAR(); \
        _Pragma("unroll") for (int ks = 0; ks < 32; ++ks) { \
            if (ks == 6) { WAIT(1); } \
            if (ks + 2 < 32) { A0[(ks + 2) % 3] = lds16((ab) + (ks + 2) * 1024); A1[(ks + 2) % 3] = lds16((ab) + 32768 + (ks + 2) * 1024); } \
            SBAR(); \
            acc0 = MFMA(asbf(wf[ks]), asbf(A0[ks % 3]), acc0); acc1 = MFMA(asbf(wf[ks]), asbf(A1[ks % 3]), acc1); SBAR(); \
            AFTER(ks); SBAR(); \
        } } while (0)
#define KLOOP1(ab, wf, acc0, WAIT, AFTER) do { \
        u32x4 A0[4]; \
        WAIT(0); \
        A0[0] = lds16((ab)); A0[1] = lds16((ab) + 1024); A0[2] = lds16((ab) + 2048); SBAR(); \
        _Pragma("unroll") for (int ks = 0; ks < 32; ++ks) { \
            if (ks == 5) { WAIT(1); } \
            if (ks + 3 < 32) A0[(ks + 3) % 4] = lds16((ab) + (ks + 3) * 1024); \
            SBAR(); \
            acc0 = MFMA(asbf(wf[ks]), asbf(A0[ks % 4]), acc0); SBAR(); \
            AFTER(ks); SBAR(); \
        } } while (0)
#define NOTHING(x) do { } while (0)
DI void load_cs(const Ctx& c, unsigned off, f32x16& cs) {
#pragma unroll
    for (int m = 0; m < 4; ++m) { const f32x4 t = __builtin_bit_cast(f32x4, ld16(c.rs, (c.lane >> 5) * 64 + m * 16, off));
        cs[4 * m] = t[0]; cs[4 * m + 1] = t[1]; cs[4 * m + 2] = t[2]; cs[4 * m + 3] = t[3]; }
}

constexpr int QKV_PRE = TRAIL ? 16 : 8;
DI unsigned wq_off(int w, int j) { return OFF_WQ + (6 * j + (w < 4 ? w : 4 + (w & 1))) * 32 * 1024; }
DI void phase_qkv1(const Ctx& c0, int s, int j, u32x4 (&wf)[32]) {
    const Ctx c = fresh(c0); const int w = c.wid; OPAQUE_S(s); OPAQUE_S(j);
    const int nb = w < 4 ? w : 4 + (w & 1);
    const unsigned wo_ = wq_off(w, j); const int rot = rot32(s, j);
    f32x16 cs; load_cs(c, OFF_CSQ + (6 * j + nb) * 128, cs);
    TSDECL; TS(0);
    await<1>(c, PH_F2, blk_chain(s, c.lane & 7));
    STAMP(4 * 6 + 1); TS(1);
    issue_a512(c, w, OFF_XT + 2 * s * 32 * 1024, OFF_ST + ST_B + s * 8192, rot);
#pragma unroll
    for (int i = QKV_PRE; i < 32; ++i) wf[i] = ld16(c.rs, c.l16, fr32(wo_, i, rot));
    const unsigned ab = c.lds0 + c.l16;
#define QKV_WAIT(n) do { WAITV_BAR(((n) == 0 ? 6 : 0) + 32 - QKV_PRE); if ((n) == 0) TS(2); else TS(3); } while (0)
    {
        const int tb = w < 4 ? 0 : (w >> 1) & 1;
        f32x16 acc = {}; float mu, rs_; RowStat st;
#define QKV_AFTER(ks) do { if ((ks) == 8) stat_read(c, tb, st); if ((ks) == 13) stat_finish(st, mu, rs_); } while (0)
        KLOOP1(ab + tb * 32768, wf, acc, QKV_WAIT, QKV_AFTER);
#undef QKV_AFTER
        qkv_store(c, acc, cs, mu, rs_, w < 4 ? w >> 1 : 2, w & 1, 1, s, j, tb);
    }
    if (w < 4) {
        f32x16 acc = {}; float mu, rs_; RowStat st;
#define QKV_AFTER(ks) do { if ((ks) == 2) stat_read(c, 1, st); if ((ks) == 7) stat_finish(st, mu, rs_); } while (0)
        KLOOP1(ab + 32768, wf, acc, NOTHING, QKV_AFTER);
#undef QKV_AFTER
        STAMP(4 * 6 + 2); TS(4);
        qkv_store(c, acc, cs, mu, rs_, w >> 1, w & 1, 1, s, j, 1);
    }
#undef QKV_WAIT
    TS(5);
    publish<0>(c, PH_QKV + PH_PER_LAYER);
    TS(7); TSFLUSH(5);
}

#define W1PRE 16
#define W1MID 10
constexpr int W1_PRE = W1PRE, W1_MID = W1MID;
DI unsigned w1_off(int w, int j) { return w < 7 ? OFF_W1 + (7 * j + w) * 32 * 1024 : OOB; }
DI void phase_attn(const Ctx& c0, int l, int lp, int s, int j, u32x4 (&wf)[32], u32x4 (&wo)[8]) {
    OPAQUE_S(lp); OPAQUE_S(l); const Ctx c = fresh(c0); const int wid = c.wid, bb = c.b; OPAQUE_S(j); OPAQUE_S(s);
    const int lane = c.lane, h = (bb & 7) + 8 * ((bb >> 3) & 3), g = bb >> 5;
    const int qp = wid & 3, kh = wid >> 2;
    TSDECL; TS(0);
    __builtin_amdgcn_s_waitcnt(waitcnt_imm(0, 15));
    for (int i = wid * 64 + lane; i < ONES_BYTES / 4; i += NT) *(LAS unsigned*)(uintptr_t)(c.lds0 + LDS_ONES + i * 4) = (i & 7) == 0 ? 0x00003f80u : 0u;
    await<1>(c, PH_QKV + lp, blk_chain(lane & 31, h >> 2));
    STAMP(4 * (2 + 5 * l) + 1); TS(1);
    const unsigned kb = OFF_K + l * ACT_B + h * 65536, vb = OFF_V + l * ACT_B + h * 65536;
    u32x4 qrA, qrB; float km;
    {
        const unsigned qo = OFF_Q + l * ACT_B + (h * 64 + 8 * g + 2 * qp) * 1024, ko = OFF_KM + (l * 32 + h) * 256, l4 = lane * 4;
        asm volatile("s_nop 4\n\tbuffer_load_dwordx4 %0, %3, %4, %5 offen sc1\n\tbuffer_load_dwordx4 %1, %3, %4, %5 offen offset:1024 sc1\n\tbuffer_load_dword %2, %6, %4, %7 offen sc1"
                     : "=&v"(qrA), "=&v"(qrB), "=&v"(km) : "v"(c.l16), "s"(c.rs), "s"(qo), "v"(l4), "s"(ko) : "memory");
    }
    {
        const unsigned sk = c.lds0 + LDS_K + kh * 32768, sv = c.lds0 + LDS_V + kh * 32768;
        const unsigned tA = 8 * g + 4 * kh + qp, i0 = 8 * g + 8 + 28 * kh;
        dma<1>(c.rs, c.l16, kb + tA * 1024, sk + qp * 1024);
        dma<1>(c.rs, c.l16, vb + tA * 1024, sv + qp * 1024);
        ORDER();
        const unsigned tB = (i0 + 2 * qp) & 63;
        dma<2>(c.rs, c.l16, kb + tB * 1024, sk + (4 + 2 * qp) * 1024);
        dma<2>(c.rs, c.l16, vb + tB * 1024, sv + (4 + 2 * qp) * 1024);
    }
    TS(6);
    asm volatile("s_waitcnt vmcnt(6)" : "+v"(qrA), "+v"(qrB), "+v"(km) :: "memory");
    TS(5);
    km = swap32_max(max32(km));
    float q2A = 0.f, q2B = 0.f;
#pragma unroll
    for (int i = 0; i < 4; ++i) { float lo = bflo(qrA[i]), hi = bfhi(qrA[i]); q2A += lo * lo + hi * hi; lo = bflo(qrB[i]); hi = bfhi(qrB[i]); q2B += lo * lo + hi * hi; }
    q2A = swap32_sum(q2A); q2B = swap32_sum(q2B);
    float mA = __builtin_amdgcn_sqrtf(q2A * km) * 1.015625f, mB = __builtin_amdgcn_sqrtf(q2B * km) * 1.015625f;
    const bf16x8 qA = asbf(qrA), qB = asbf(qrB);
    const int grp = lane >> 4, h2 = grp >> 1, isone = grp & 1, qq = (lane & 15) >> 2, pp = lane & 3;
    const int inrow = qq * 32 + (pp & 1) * 16 + (pp >> 1) * 8;
    const int vstep = isone ? 0 : ATT_UNR * 1024, vstep4 = isone ? 0 : 4096;
    for (int pass = 0;; ++pass) {
        f32x16 nmA, nmB;
#pragma unroll
        for (int i = 0; i < 16; ++i) { nmA[i] = -mA; nmB[i] = -mB; }
        asm volatile("" : "+v"(nmA), "+v"(nmB));
        unsigned kpa = c.lds0 + LDS_K + kh * 32768 + c.l16; asm volatile("" : "+v"(kpa));
        unsigned vpa = c.lds0 + (isone ? LDS_ONES + (h2 ? 0 : 128) + inrow : LDS_V + kh * 32768 + h2 * 128 + inrow); asm volatile("" : "+v"(vpa));
        f32x16 oA = {}, oB = {};
#define KRD(t) asbf(lds16(kpa + (t) * 1024))
#define VTR(off) ldstr(vpa + (off))
        WAITV_BAR(4);
        TS(2);
        bf16x8 kf = KRD(0);
        s16x4 va0 = VTR(0), va1 = VTR(256), vb0 = VTR(512), vb1 = VTR(768);
        f32x16 sA = MFMA(kf, qA, nmA), sB;
        u32x4 p0, p1;
#define EX(cc, i) do { cc[i] = __builtin_amdgcn_exp2f(cc[i]); SBAR(); } while (0)
#define CV(p, jj, cc, i) do { p[jj] = cvtpk(cc[i], cc[i + 1]); SBAR(); } while (0)
#define STREAM(cur, ocur, prv, oprv, qprv, nmprv, tl, ISA, PEND) do { \
        SBAR(); \
        EX(cur, 0); EX(cur, 1); if (PEND) CV(p1, 2, prv, 12); EX(cur, 2); if (PEND) CV(p1, 3, prv, 14); EX(cur, 3); EX(cur, 4); \
        if (PEND) { oprv = MFMA(cat(vb0, vb1), asbf(p1), oprv); asm volatile("" : "+v"(oprv)); SBAR(); } \
        if (ISA && PEND) { vb0 = VTR((tl) * 1024 + 512); vb1 = VTR((tl) * 1024 + 768); SBAR(); } \
        CV(p0, 0, cur, 0); EX(cur, 5); EX(cur, 6); CV(p0, 1, cur, 2); EX(cur, 7); EX(cur, 8); \
        prv = MFMA(kf, qprv, nmprv); asm volatile("" : "+v"(prv)); SBAR(); \
        if (ISA) { kf = KRD((tl) + 1); SBAR(); } \
        CV(p0, 2, cur, 4); EX(cur, 9); EX(cur, 10); CV(p0, 3, cur, 6); EX(cur, 11); EX(cur, 12); \
        ocur = MFMA(cat(va0, va1), asbf(p0), ocur); asm volatile("" : "+v"(ocur)); SBAR(); \
        if (!(ISA)) { va0 = VTR(((tl) + 1) * 1024); va1 = VTR(((tl) + 1) * 1024 + 256); SBAR(); } \
        CV(p1, 0, cur, 8); EX(cur, 13); EX(cur, 14); CV(p1, 1, cur, 10); EX(cur, 15); \
    } while (0)
#define PAIR(tl, PEND) do { STREAM(sA, oA, sB, oB, qB, nmB, tl, true, PEND); STREAM(sB, oB, sA, oA, qA, nmA, tl, false, true); } while (0)
#define BUMP(n, vs) do { kpa += (n) * 1024; vpa += (vs); asm volatile("" : "+v"(kpa), "+v"(vpa)); } while (0)
        PAIR(0, false);
        PAIR(1, true);
        PAIR(2, true);
        WAITV_BAR(0);
        if (pass == 0) {
            int w_ = wid, b_ = bb, l_ = l; OPAQUE_S(w_); OPAQUE_S(b_); OPAQUE_S(l_);
            const int qp_ = w_ & 3, kh_ = w_ >> 2;
            const unsigned sk = c.lds0 + LDS_K + kh_ * 32768 + (12 + 5 * qp_) * 1024, i0 = 8 * (b_ >> 5) + 16 + 28 * kh_ + 5 * qp_;
            const unsigned kb_ = OFF_K + l_ * ACT_B + ((b_ & 7) + 8 * ((b_ >> 3) & 3)) * 65536;
#pragma unroll
            for (int i = 0; i < 5; ++i) { const unsigned tC = (i0 + i) & 63;
                dma<1>(c.rs, c.l16, kb_ + tC * 1024, sk + i * 1024);
                dma<1>(c.rs, c.l16, kb_ + (OFF_V - OFF_K) + tC * 1024, sk + (LDS_V - LDS_K) + i * 1024); }
        }
        PAIR(3, true);
        BUMP(4, vstep4);
#pragma unroll
        for (int u = 0; u < ATT_UNR; ++u) PAIR(u, true);
        BUMP(ATT_UNR, vstep);
        WAITV_BAR(0);
        if (l == 0) await<4>(c, PH_PREP, lane);
        STAMP(4 * (2 + 5 * l) + 0); TS(3);
        if (l == 0) WSTAMP(64);
        int w_ = wid, j_ = j, s_ = s; OPAQUE_S(w_); OPAQUE_S(j_); OPAQUE_S(s_);
        const unsigned o1 = OFF_WO + (2 * j_ + (w_ & 1)) * 32 * 1024, o2 = w1_off(w_, j_); const int rot_ = rot32(s_, j_), q4_ = 4 * (w_ >> 1);
#define PF(i) do { if ((i) < 8) wo[(i) & 7] = ld16(c.rs, c.l16, fr32(o1, 16 * (((i) & 7) >> 2) + ((i) & 3) + q4_, rot_)); \
        else if ((i) < 8 + W1_PRE) wf[((i) - 8) & 31] = ld16(c.rs, c.l16, fr32(o2, (i) - 8, rot_)); SBAR(); } while (0)
#pragma unroll
        for (int blk = 0; blk < 3; ++blk) {
#pragma unroll
            for (int u = 0; u < ATT_UNR; ++u) {
                STREAM(sA, oA, sB, oB, qB, nmB, u, true, true);  PF(2 * (ATT_UNR * blk + u));
                STREAM(sB, oB, sA, oA, qA, nmA, u, false, true); PF(2 * (ATT_UNR * blk + u) + 1);
            }
            BUMP(ATT_UNR, vstep);
        }
#undef PF
        CV(p1, 2, sB, 12); CV(p1, 3, sB, 14);
        oB = MFMA(cat(vb0, vb1), asbf(p1), oB);
        STAMP(4 * (2 + 5 * l) + 2); TS(4);
        if (l == 0) WSTAMP(56);
        {
            const f32x16 give = kh ? oA : oB;
            const unsigned xb = c.lds0 + LDS_XCH + wid * 2304;
            *(LAS f32x4*)(uintptr_t)(xb + c.l16) = (f32x4){give[0], give[1], give[2], give[3]};
            *(LAS f32x4*)(uintptr_t)(xb + 1024 + c.l16) = (f32x4){give[4], give[5], give[6], give[7]};
            *(LAS float*)(uintptr_t)(xb + 2048 + lane * 4) = give[8];
            if (wid == 0 && lane == 0) *(LAS unsigned*)(uintptr_t)(c.lds0 + LDS_FLAG) = 0u;
        }
        BAR();
        f32x16 o = kh ? oB : oA;
        {
            const unsigned xb = c.lds0 + LDS_XCH + (wid ^ 4) * 2304;
            const f32x4 u0 = ldsf4(xb + c.l16), u1 = ldsf4(xb + 1024 + c.l16);
            const float ul = *(const LAS float*)(uintptr_t)(xb + 2048 + lane * 4);
#pragma unroll
            for (int i = 0; i < 4; ++i) { o[i] += u0[i]; o[4 + i] += u1[i]; }
            o[8] = swap32_lo(o[8] + ul);
        }
        const float rl = __builtin_amdgcn_rcpf(o[8]);
        int w2_ = wid, b2_ = bb, l2_ = l; OPAQUE_S(w2_); OPAQUE_S(b2_); OPAQUE_S(l2_);
        const unsigned o_off = OFF_O + l2_ * ACT_B + ((8 * (b2_ >> 5) + 2 * (w2_ & 3) + (w2_ >> 2)) * 32 + (b2_ & 7) + 8 * ((b2_ >> 3) & 3)) * 1024;
        st16(c.rs, (u32x4){cvtpk(o[0] * rl, o[1] * rl), cvtpk(o[2] * rl, o[3] * rl), cvtpk(o[4] * rl, o[5] * rl), cvtpk(o[6] * rl, o[7] * rl)}, c.l16, o_off);
        const bool bad = !(o[8] >= 8.673617379884035e-19f   && o[8] <= 3.0e38f);
        if (pass == 0 && __any(bad) && lane == 0) *(LAS unsigned*)(uintptr_t)(c.lds0 + LDS_FLAG) = 1u;
        WAITV_BAR(0);
        TS(7);
        if (pass || __builtin_amdgcn_readfirstlane(*(const LAS unsigned*)(uintptr_t)(c.lds0 + LDS_FLAG)) == 0u) break;
        mA = -3.0e38f; mB = -3.0e38f;
        const f32x16 zero = {};
        for (int t = 0; t < 64; ++t) {
            const bf16x8 kk = asbf(lds16(c.lds0 + LDS_K + t * 1024 + c.l16));
            const f32x16 a = MFMA(kk, qA, zero), bq = MFMA(kk, qB, zero);
#pragma unroll
            for (int i = 0; i < 16; ++i) { mA = fmaxf(mA, a[i]); mB = fmaxf(mB, bq[i]); }
        }
        mA = swap32_max(mA); mB = swap32_max(mB);
        BAR();
#undef KRD
#undef VTR
#undef EX
#undef CV
#undef STREAM
#undef PAIR
#undef BUMP
    }
    TSFLUSH(l == 0 ? 1 : 6);
    if (c.wid == 0 && c.lane == 0) { u64 t = c.tag; asm volatile("" : "+s"(t)); t += (u64)(PH_ATT + lp);
        st8(c.rs, (u32x2){(unsigned)t, (unsigned)(t >> 32)}, 0, OFF_FLAGS + ((PH_ATT + lp) * 256 + c.b) * 128); }
}

DI void res_finish(const Ctx& c, const Params& p, int w, int s, int j, bool last, const f32x16& v, unsigned x_off, unsigned st_off, unsigned (&xres)[8]) {
    const int lane = c.lane, tb = w >> 1, nb = w & 1;
    f32x2 y[8];
#pragma unroll
    for (int m = 0; m < 8; ++m) { y[m] = (f32x2){bflo(xres[m]), bfhi(xres[m])} + (f32x2){v[2 * m], v[2 * m + 1]}; xres[m] = cvtpk(y[m][0], y[m][1]); }
    if (last) {
        const rsrc_t ro = mkrsrc(p.out, 2048 * 512 * 2);
        const unsigned vo = (64 * s + 32 * tb + (lane & 31)) * 1024 + (64 * j + 32 * nb) * 2 + (lane >> 5) * 16;
#pragma unroll
        for (int k = 0; k < 2; ++k) {
            const auto a = __builtin_amdgcn_permlane32_swap(xres[4 * k], xres[4 * k + 2], false, false);
            const auto b = __builtin_amdgcn_permlane32_swap(xres[4 * k + 1], xres[4 * k + 3], false, false);
            __builtin_amdgcn_raw_buffer_store_b128((u32x4){a[0], b[0], a[1], b[1]}, ro, vo + 32 * k, 0, 0);
        }
        return;
    }
    const unsigned base = x_off + ((2 * s + tb) * 32 + 4 * j + 2 * nb) * 1024;
    st16(c.rs, (u32x4){xres[0], xres[1], xres[2], xres[3]}, c.l16, base);
    st16(c.rs, (u32x4){xres[4], xres[5], xres[6], xres[7]}, c.l16, base + 1024);
    f32x2 s2 = (y[0] + y[1]) + (y[2] + y[3]), q2 = y[0] * y[0];
    s2 += (y[4] + y[5]) + (y[6] + y[7]);
#pragma unroll
    for (int m = 1; m < 8; ++m) q2 += y[m] * y[m];
    const float sm = swap32_sum(s2[0] + s2[1]), sq = swap32_sum(q2[0] + q2[1]);
    if (lane < 32) st8(c.rs, (u32x2){__float_as_uint(sm), __float_as_uint(sq)}, lane * 128, st_off + (64 * s + 32 * tb) * 128 + (2 * j + nb) * 8);
}
DI void put16(unsigned a, const f32x16& v) {
#pragma unroll
    for (int m = 0; m < 4; ++m) *(LAS f32x4*)(uintptr_t)(a + m * 1024) = (f32x4){v[4 * m], v[4 * m + 1], v[4 * m + 2], v[4 * m + 3]};
}
DI void add16x3(unsigned a, f32x16& v) {
    f32x4 t[12];
#pragma unroll
    for (int m = 0; m < 12; ++m) t[m] = ldsf4(a + m * 1024);
#pragma unroll
    for (int m = 0; m < 4; ++m) { const f32x4 u = (t[m] + t[4 + m]) + t[8 + m]; v[4 * m] += u[0]; v[4 * m + 1] += u[1]; v[4 * m + 2] += u[2]; v[4 * m + 3] += u[3]; }
}

DI void parts_out(unsigned base, int nb, int q, int tbA, const f32x16& acc0, const f32x16& acc1) {
    const int sl = q > 0 ? q - 1 : 0;
    put16(base + ((nb + 2 * (1 - tbA)) * 3 + sl) * 4096, acc1);
    if (q >= 2) put16(base + (nb * 3 + sl) * 4096, acc0);
}
DI void phase_wo(const Ctx& c0, const Params& p, int l, int lp, int s, int j, unsigned (&xres)[8], u32x4 (&wo)[8], u32x4 (&wf)[32]) {
    OPAQUE_S(lp); OPAQUE_S(l); const Ctx c = fresh(c0); const int w = c.wid; OPAQUE_S(s); OPAQUE_S(j);
    const int nb = w & 1, q = w >> 1, tbA = q == 1 ? 1 : 0;
    {
        const int lane = c.lane, tb = (w >> 1) & 1;
        if (l == 0) {
            const rsrc_t rx = mkrsrc(p.x, 2048 * 512 * 2);
            const unsigned vo = tb * 32768 + (lane & 31) * 1024 + (64 * j + 32 * nb + 4 * (lane >> 5)) * 2;
#pragma unroll
            for (int m = 0; m < 4; ++m) { const u32x2 t = __builtin_amdgcn_raw_buffer_load_b64(rx, vo + 16 * m, s * 65536, 0); xres[2 * m] = t[0]; xres[2 * m + 1] = t[1]; }
        } else {
            const unsigned o = OFF_XT + ((2 * s + tb) * 32 + 4 * j + 2 * nb) * 1024;
            const u32x4 a = ld16(c.rs, c.l16, o), b = ld16(c.rs, c.l16, o + 1024);
#pragma unroll
            for (int m = 0; m < 4; ++m) { xres[m] = a[m]; xres[4 + m] = b[m]; }
        }
    }
    TSDECL; TS(0);
    await<1>(c, PH_ATT + lp, blk_attn(c.lane & 31, s >> 2));
    STAMP(4 * (3 + 5 * l) + 1); TS(1);
    const unsigned a_off = OFF_O + l * ACT_B + 2 * s * 32 * 1024;
    const int rot = rot32(s, j);
#define ISSUE(n) do { const unsigned o_ = 16 * ((n) >> 1) + 4 * (w & 3) + 2 * ((n) & 1); \
        dma<2>(c.rs, c.l16, fr32(a_off + (w >> 2) * 32768, o_, rot), c.lds0 + (w >> 2) * 32768 + o_ * 1024); } while (0)
    ISSUE(0); ORDER(); ISSUE(1); ISSUE(2); ISSUE(3); ORDER();
#undef ISSUE
    const unsigned nxt = w1_off(w, j);
#pragma unroll
    for (int i = W1_PRE; i < W1_PRE + W1_MID; ++i) wf[i] = ld16(c.rs, c.l16, fr32(nxt, i, rot));
    f32x16 acc0 = {}, acc1 = {};
    const unsigned ab0 = c.lds0 + c.l16 + q * 4096 + tbA * 32768, ab1 = c.lds0 + c.l16 + q * 4096 + (1 - tbA) * 32768;
#define COMPUTE(n) do { const unsigned o_ = (16 * ((n) >> 1) + 2 * ((n) & 1)) * 1024; u32x4 A0[2], A1[2]; \
        _Pragma("unroll") for (int i = 0; i < 2; ++i) { A0[i] = lds16(ab0 + o_ + i * 1024); A1[i] = lds16(ab1 + o_ + i * 1024); } SBAR(); \
        _Pragma("unroll") for (int i = 0; i < 2; ++i) { \
            acc0 = MFMA(asbf(wo[2 * (n) + i]), asbf(A0[i]), acc0); acc1 = MFMA(asbf(wo[2 * (n) + i]), asbf(A1[i]), acc1); SBAR(); } } while (0)
    WAITV_BAR(6 + W1_MID); TS(2); COMPUTE(0);
    WAITV_BAR(W1_MID); COMPUTE(1); COMPUTE(2); COMPUTE(3);
#undef COMPUTE
    STAMP(4 * (3 + 5 * l) + 2); TS(3);
    parts_out(c.lds0 + 65536 + c.l16, nb, q, tbA, acc0, acc1);
    BAR();
    TS(4);
    if (w < 4) {
        const unsigned xa = c.lds0 + 65536 + w * 3 * 4096 + c.l16;
        add16x3(xa, acc0);
        res_finish(c, p, w, s, j, false, acc0, OFF_X1 + l * ACT_B, OFF_ST + (2 * l) * ST_B, xres);
    }
    TS(5);
    publish<0>(c, PH_WO + lp);
    TS(6);
#pragma unroll
    for (int i = W1_PRE + W1_MID; i < 32; ++i) wf[i] = ld16(c.rs, c.l16, fr32(nxt, i, rot));
    TS(7); if (l == 0) TSFLUSH(2);
}

constexpr int W2_MID = 20;
DI unsigned w2_off(int w, int j) { return OFF_W2 + ((2 * j + (w & 1)) * 112 + 4 * (w >> 1)) * 1024; }
DI void phase_f1(const Ctx& c0, int l, int lp, int s, int j, u32x4 (&wf)[32]) {
    OPAQUE_S(lp); OPAQUE_S(l); const Ctx c = fresh(c0); const int w = c.wid; OPAQUE_S(s); OPAQUE_S(j);
    const int nb = 7 * j + w;
    f32x16 cs; load_cs(c, w < 7 ? OFF_CS1 + nb * 128 : OOB, cs);
    TSDECL; TS(0);
    await<1>(c, PH_WO + lp, blk_chain(s, c.lane & 7));
    TS(1);
    STAMP(4 * (4 + 5 * l) + 1);
    if (l == 0) XST(79);
    const int rot = rot32(s, j), r7 = rot7(s, j);
    issue_a512(c, w, OFF_X1 + l * ACT_B + 2 * s * 32 * 1024, OFF_ST + (2 * l) * ST_B + s * 8192, rot);
    constexpr int LATE1 = TRAIL ? 0 : 32 - W1_PRE - W1_MID;
    const unsigned ab = c.lds0 + c.l16, nxt = w2_off(w, j);
#define W2FRAG(i) ld16(c.rs, c.l16, nxt + (16 * chunk7((i) >> 2, r7) + ((i) & 3)) * 1024)
#define F1_WAIT(n) do { WAITV_BAR(((n) == 0 ? 6 : 0) + LATE1); if (l == 0) XST(80 + (n)); if ((n) == 0) TS(2); if ((n) == 1) TS(3); } while (0)
#define F1_OUT(tb) do { float z[16]; \
        _Pragma("unroll") for (int i = 0; i < 16; ++i) z[i] = (acc[i] - mu * cs[i]) * rs_; \
        const unsigned base = w < 7 ? OFF_H + l * H_B + ((2 * s + (tb)) * 112 + 2 * nb) * 1024 : OOB; \
        st16(c.rs, (u32x4){relupk(z[0], z[1]), relupk(z[2], z[3]), relupk(z[4], z[5]), relupk(z[6], z[7])}, c.l16, base); \
        st16(c.rs, (u32x4){relupk(z[8], z[9]), relupk(z[10], z[11]), relupk(z[12], z[13]), relupk(z[14], z[15])}, c.l16, base + 1024); } while (0)
    {
        f32x16 acc = {}; float mu, rs_; RowStat st;
#define F1_AFTER(ks) do { if ((ks) == 8) stat_read(c, 0, st); if ((ks) == 13) stat_finish(st, mu, rs_); } while (0)
        KLOOP1(ab, wf, acc, F1_WAIT, F1_AFTER);
#undef F1_AFTER
        F1_OUT(0);
    }
    {
        f32x16 acc = {}; float mu, rs_; RowStat st;
#define F1_AFTER(ks) do { if ((ks) < W2_MID) wf[ks] = W2FRAG(ks); if ((ks) == 22) stat_read(c, 1, st); if ((ks) == 27) stat_finish(st, mu, rs_); } while (0)
        KLOOP1(ab + 32768, wf, acc, NOTHING, F1_AFTER);
#undef F1_AFTER
        TS(4);
        STAMP(4 * (4 + 5 * l) + 2);
        F1_OUT(1);
    }
#undef F1_WAIT
#undef F1_OUT
    TS(5);
    publish<0>(c, PH_F1 + lp);
    TS(6);
#pragma unroll
    for (int i = W2_MID; i < 28; ++i) wf[i] = W2FRAG(i);
#undef W2FRAG
    TS(7); if (l == 0) TSFLUSH(3);
}

DI void phase_f2(const Ctx& c0, const Params& p, int l, int lp, int s, int j, unsigned (&xres)[8], u32x4 (&wf)[32]) {
    OPAQUE_S(lp); OPAQUE_S(l); const Ctx c = fresh(c0); const int w = c.wid; OPAQUE_S(s); OPAQUE_S(j);
    const int nb = w & 1, q = w >> 1, tbA = q == 1 ? 1 : 0;
    const bool last = l != 0;
    TSDECL; TS(0);
    await<1>(c, PH_F1 + lp, blk_chain(s, c.lane & 7));
    STAMP(4 * (5 + 5 * l) + 1); TS(1);
    const unsigned a_off = OFF_H + l * H_B + 2 * s * 112 * 1024;
    const int r7 = rot7(s, j), rot = rot32(s, j);
#define ISSUE(ch) dma<4>(c.rs, c.l16, a_off + ((w >> 2) * 112 + 16 * chunk7(ch, r7) + 4 * (w & 3)) * 1024, c.lds0 + ((ch) & 3) * 32768 + (w >> 2) * 16384 + 4 * (w & 3) * 1024)
    f32x16 acc0 = {}, acc1 = {};
    const unsigned ab0 = c.lds0 + c.l16 + q * 4096 + tbA * 16384, ab1 = c.lds0 + c.l16 + q * 4096 + (1 - tbA) * 16384;
#define COMPUTE(ch) do { const unsigned o_ = ((ch) & 3) * 32768; u32x4 A0[4], A1[4]; \
        _Pragma("unroll") for (int i = 0; i < 2; ++i) { A0[i] = lds16(ab0 + o_ + i * 1024); A1[i] = lds16(ab1 + o_ + i * 1024); } SBAR(); \
        _Pragma("unroll") for (int i = 0; i < 4; ++i) { \
            if (i < 2) { A0[i + 2] = lds16(ab0 + o_ + (i + 2) * 1024); A1[i + 2] = lds16(ab1 + o_ + (i + 2) * 1024); } SBAR(); \
            acc0 = MFMA(asbf(wf[4 * (ch) + i]), asbf(A0[i]), acc0); acc1 = MFMA(asbf(wf[4 * (ch) + i]), asbf(A1[i]), acc1); SBAR(); } } while (0)
    const unsigned nxt = last ? OOB : wq_off(w, j);
    constexpr int P = 8;
    ISSUE(0); ORDER(); ISSUE(1); ORDER(); ISSUE(2); ISSUE(3); ORDER();
    constexpr int R = TRAIL ? 0 : 28 - W2_MID;
    WAITV_BAR(12 + R); TS(2); COMPUTE(0);
    WAITV_BAR(8 + R); ISSUE(4); COMPUTE(1);
    WAITV_BAR(4 + R + 4); ISSUE(5); COMPUTE(2);
    WAITV_BAR(R + 8); ISSUE(6); COMPUTE(3);
#pragma unroll
    for (int i = 0; i < P; ++i) wf[i] = ld16(c.rs, c.l16, fr32(nxt, i, rot));
    WAITV_BAR(8 + P); COMPUTE(4);
    WAITV_BAR(4 + P); COMPUTE(5);
    WAITV_BAR(P); TS(3); COMPUTE(6);
#undef ISSUE
#undef COMPUTE
    STAMP(4 * (5 + 5 * l) + 2);
    parts_out(c.lds0 + c.l16, nb, q, tbA, acc0, acc1);
    BAR();
    TS(4);
    if (w < 4) {
        const unsigned xa = c.lds0 + w * 3 * 4096 + c.l16;
        add16x3(xa, acc0);
        res_finish(c, p, w, s, j, last, acc0, OFF_XT, OFF_ST + ST_B, xres);
    }
    if (last) { __builtin_amdgcn_s_waitcnt(waitcnt_imm(0, 15)); return; }
    TS(5);
    publish<0>(c, PH_F2);
    TS(6);
#pragma unroll
    for (int i = P; i < QKV_PRE; ++i) wf[i] = ld16(c.rs, c.l16, fr32(nxt, i, rot));
    TS(7); TSFLUSH(4);
}

__global__ void __launch_bounds__(NT, 2) fused_layers_kernel(Params p) {
    extern __shared__ __attribute__((aligned(16))) char lds[];
    Ctx c;
    c.ws = (GAS char*)p.ws; c.rs = mkrsrc(p.ws, WS_BYTES);
    c.lds0 = (unsigned)(uintptr_t)(LAS char*)lds;
    c.lane = threadIdx.x & 63; c.wid = __builtin_amdgcn_readfirstlane(threadIdx.x >> 6); c.l16 = c.lane * 16; c.b = blockIdx.x;
    c.tag = 0;
    const int x = c.b & 7, r = c.b >> 3, s = 4 * x + (r >> 3), j = r & 7;
    {
        u64 pc; asm volatile("s_getpc_b64 %0" : "=s"(pc));
        if (c.wid == 7) warm4k(mkrsrc((const void*)(pc & ~(u64)63), 37 * 1024), r < 10 ? r * 4096 + c.lane * 64 : OOB, 0, c.lds0 + LDS_DUMMY);
    }
    unsigned xres[8];
    u32x4 wf[32], wo[8];
    ESTAMP(0);
    phase_first(c, p, s, j);
    ESTAMP(7);
#pragma unroll 1
    for (int l_ = 0;; ++l_) {
        int l = __builtin_amdgcn_readfirstlane(l_); OPAQUE_S(l);
        int lp = PH_PER_LAYER * l; OPAQUE_S(lp);
        phase_attn(c, l, lp, s, j, wf, wo);
        ESTAMP(4 * (2 + 5 * l) + 3);
        phase_wo(c, p, l, lp, s, j, xres, wo, wf);
        ESTAMP(4 * (3 + 5 * l) + 3);
        phase_f1(c, l, lp, s, j, wf);
        ESTAMP(4 * (4 + 5 * l) + 3);
        phase_f2(c, p, l, lp, s, j, xres, wf);
        ESTAMP(4 * (5 + 5 * l) + 3);
        if (l != 0) break;
        phase_qkv1(c, s, j, wf);
        ESTAMP(4 * 6 + 3);
    }
    const Ctx ce = fresh(c);
    if (ce.b == 0 && ce.wid == 0 && ce.lane == 0) { const u64 t = ce.tag + 15; st8(ce.rs, (u32x2){(unsigned)t, (unsigned)(t >> 32)}, 0, OFF_EPOCH); }
}

}

extern "C" void kernel_launch(void* const* d_in, const int* in_sizes, int n_in, void* d_out, int out_size, void* d_ws, size_t ws_size, hipStream_t stream) {
    (void)in_sizes; (void)n_in; (void)out_size; (void)ws_size;
    Params p;
    p.x = d_in[0]; p.wqkv = d_in[1]; p.wo = d_in[2]; p.w1 = d_in[4]; p.w2 = d_in[5]; p.out = d_out; p.ws = d_ws;
    (void)hipFuncSetAttribute((const void*)fused_layers_kernel, hipFuncAttributeMaxDynamicSharedMemorySize, LDS_TOTAL);
    hipLaunchKernelGGL(fused_layers_kernel, dim3(256), dim3(NT), LDS_TOTAL, stream, p);
}
```

```cpp
#include <hip/hip_runtime.h>
#include <hip/hip_bf16.h>
#include <stdint.h>

namespace {

#define GAS __attribute__((address_space(1)))
#define LAS __attribute__((address_space(3)))
#define DI __device__ __forceinline__
typedef __bf16 bf16x8 __attribute__((ext_vector_type(8)));
typedef __bf16 bf16x2 __attribute__((ext_vector_type(2)));
typedef short s16x4 __attribute__((ext_vector_type(4)));
typedef float f32x2 __attribute__((ext_vector_type(2)));
typedef float f32x4 __attribute__((ext_vector_type(4)));
typedef float f32x16 __attribute__((ext_vector_type(16)));
typedef unsigned u32x2 __attribute__((ext_vector_type(2)));
typedef unsigned u32x4 __attribute__((ext_vector_type(4)));
typedef unsigned long long u64;
typedef __amdgpu_buffer_rsrc_t rsrc_t;

#define P0TOUCH 0
#define TRAIL 1
#define WARM 0
constexpr int NT = 512;
constexpr unsigned OFF_EPOCH = 0;
constexpr unsigned OFF_FLAGS = 0x1000;
constexpr unsigned OFF_PROF  = 0x2D00000;
constexpr unsigned OFF_CSQ   = 0x70000;
constexpr unsigned OFF_CS1   = 0x72000;
constexpr unsigned OFF_KM    = 0x74000;
constexpr unsigned OFF_ST    = 0x2C00000;
constexpr unsigned OFF_WQ    = 0x100000;
constexpr unsigned OFF_WO    = 0x280000;
constexpr unsigned OFF_W1    = 0x300000;
constexpr unsigned OFF_W2    = 0x4C0000;
constexpr unsigned OFF_XT    = 0x700000;
constexpr unsigned OFF_Q     = 0xA00000;
constexpr unsigned OFF_K     = 0xE00000;
constexpr unsigned OFF_V     = 0x1200000;
constexpr unsigned OFF_O     = 0x1600000;
constexpr unsigned OFF_X1    = 0x1A00000;
constexpr unsigned OFF_H     = 0x1E00000;
constexpr unsigned WS_BYTES  = 0x2E80000;
constexpr unsigned ACT_B = 0x200000, H_B = 0x700000, ST_B = 0x40000;
constexpr int PH_PREP = 0, PH_QKV = 1, PH_ATT = 2, PH_WO = 3, PH_F1 = 4, PH_F2 = 5, PH_PER_LAYER = 5;
constexpr int ATT_UNR = 7;
constexpr int LDS_K = 0, LDS_V = 65536, LDS_ONES = 131072, ONES_BYTES = (ATT_UNR + 2) * 1024 + 256;
constexpr int LDS_XCH = LDS_ONES + ONES_BYTES;
constexpr int LDS_FLAG = LDS_XCH + 8 * 2304;
constexpr int LDS_DUMMY = LDS_FLAG + 64;
constexpr int LDS_TOTAL = LDS_DUMMY + 4096;
static_assert(LDS_TOTAL <= 163840, "LDS");

struct Params { const void* x; const void* wqkv; const void* wo; const void* w1; const void* w2; void* out; void* ws; };

#define MFMA(a, b, c) __builtin_amdgcn_mfma_f32_32x32x16_bf16((a), (b), (c), 0, 0, 0)
#define SBAR() __builtin_amdgcn_sched_barrier(0)
#define BAR() asm volatile("s_waitcnt lgkmcnt(0)\n\ts_barrier" ::: "memory")
constexpr int waitcnt_imm(int vm, int lgkm) { return (vm & 15) | ((vm >> 4) << 14) | (7 << 4) | (lgkm << 8); }
#define WAITV_BAR(n) do { asm volatile("" ::: "memory"); __builtin_amdgcn_s_waitcnt(waitcnt_imm(n, 0)); asm volatile("s_barrier" ::: "memory"); } while (0)
#define ORDER() asm volatile("s_barrier" ::: "memory")
#define DRAIN() asm volatile("s_waitcnt vmcnt(0)" ::: "memory")

DI unsigned cvtpk(float lo, float hi) { f32x2 v = {lo, hi}; return __builtin_bit_cast(unsigned, __builtin_convertvector(v, bf16x2)); }
DI unsigned relupk(float lo, float hi) { typedef short s16x2 __attribute__((ext_vector_type(2)));
    const s16x2 v = __builtin_bit_cast(s16x2, cvtpk(lo, hi)); return __builtin_bit_cast(unsigned, __builtin_elementwise_max(v, (s16x2){0, 0})); }
DI bf16x8 asbf(u32x4 v) { return __builtin_bit_cast(bf16x8, v); }
DI float bflo(unsigned u) { return __uint_as_float(u << 16); }
DI float bfhi(unsigned u) { return __uint_as_float(u & 0xffff0000u); }
DI rsrc_t mkrsrc(const void* p, unsigned bytes) { return __builtin_amdgcn_make_buffer_rsrc((void*)p, 0, bytes, 0x00020000); }
DI u32x4 ld16(rsrc_t rs, unsigned voff, unsigned soff) { return __builtin_amdgcn_raw_buffer_load_b128(rs, voff, soff, 16); }
DI unsigned ld4(rsrc_t rs, unsigned voff, unsigned soff) { return __builtin_amdgcn_raw_buffer_load_b32(rs, voff, soff, 16); }
DI void st16(rsrc_t rs, u32x4 v, unsigned voff, unsigned soff) { __builtin_amdgcn_raw_buffer_store_b128(v, rs, voff + soff, 0, 16); }
DI void st8(rsrc_t rs, u32x2 v, unsigned voff, unsigned soff) { __builtin_amdgcn_raw_buffer_store_b64(v, rs, voff, soff, 16); }
DI void st4(rsrc_t rs, unsigned v, unsigned voff, unsigned soff) { __builtin_amdgcn_raw_buffer_store_b32(v, rs, voff, soff, 16); }
#define DMA_HEAD "s_nop 2\n\ts_mov_b32 m0, %0\n\ts_nop 0\n\t"
#define DMA_ONE(off, pol) "buffer_load_dwordx4 %1, %2, %3 offen offset:" #off pol " lds\n\t"
template <int N> DI void dma(rsrc_t rs, unsigned voff, unsigned soff, unsigned ldsdst) {
    if constexpr (N == 1) asm volatile(DMA_HEAD DMA_ONE(0, " sc1") :: "s"(ldsdst), "v"(voff), "s"(rs), "s"(soff) : "memory");
    if constexpr (N == 2) asm volatile(DMA_HEAD DMA_ONE(0, " sc1") DMA_ONE(1024, " sc1") :: "s"(ldsdst), "v"(voff), "s"(rs), "s"(soff) : "memory");
    if constexpr (N == 3) asm volatile(DMA_HEAD DMA_ONE(0, " sc1") DMA_ONE(1024, " sc1") DMA_ONE(2048, " sc1") :: "s"(ldsdst), "v"(voff), "s"(rs), "s"(soff) : "memory");
    if constexpr (N == 4) asm volatile(DMA_HEAD DMA_ONE(0, " sc1") DMA_ONE(1024, " sc1") DMA_ONE(2048, " sc1") DMA_ONE(3072, " sc1") :: "s"(ldsdst), "v"(voff), "s"(rs), "s"(soff) : "memory");
}
DI void dma_in(rsrc_t rs, unsigned voff, unsigned soff, unsigned ldsdst) {
    asm volatile(DMA_HEAD DMA_ONE(0, "") :: "s"(ldsdst), "v"(voff), "s"(rs), "s"(soff) : "memory");
}
DI void warm4k(rsrc_t rs, unsigned l64, unsigned soff, unsigned ldsdst) {
    asm volatile("s_nop 2\n\ts_mov_b32 m0, %0\n\ts_nop 0\n\tbuffer_load_dword %1, %2, %3 offen lds" :: "s"(ldsdst), "v"(l64), "s"(rs), "s"(soff) : "memory");
}
#define OPAQUE_S(x) asm volatile("" : "+s"(x))
#define PIN4(a, i) asm volatile("" : "+v"(a[i]), "+v"(a[(i) + 1]), "+v"(a[(i) + 2]), "+v"(a[(i) + 3]))
DI u32x4 lds16(unsigned a) { return *(const LAS u32x4*)(uintptr_t)a; }
DI f32x4 ldsf4(unsigned a) { return *(const LAS f32x4*)(uintptr_t)a; }
DI s16x4 ldstr(unsigned a) { return __builtin_amdgcn_ds_read_tr16_b64_v4i16((LAS s16x4*)(uintptr_t)a); }
DI bf16x8 cat(s16x4 a, s16x4 b) { return __builtin_bit_cast(bf16x8, __builtin_shufflevector(a, b, 0, 1, 2, 3, 4, 5, 6, 7)); }
template <int CTRL> DI float dpp(float x) { return __builtin_bit_cast(float, __builtin_amdgcn_mov_dpp(__builtin_bit_cast(int, x), CTRL, 0xf, 0xf, true)); }
DI float swap32_sum(float x) { auto r = __builtin_amdgcn_permlane32_swap(__float_as_uint(x), __float_as_uint(x), false, false); return __uint_as_float(r[0]) + __uint_as_float(r[1]); }
DI float swap32_max(float x) { auto r = __builtin_amdgcn_permlane32_swap(__float_as_uint(x), __float_as_uint(x), false, false); return fmaxf(__uint_as_float(r[0]), __uint_as_float(r[1])); }
DI float swap32_lo(float x) { auto r = __builtin_amdgcn_permlane32_swap(__float_as_uint(x), __float_as_uint(x), false, false); return __uint_as_float(r[0]); }
DI float swap16_max(float x) { auto r = __builtin_amdgcn_permlane16_swap(__float_as_uint(x), __float_as_uint(x), false, false); return fmaxf(__uint_as_float(r[0]), __uint_as_float(r[1])); }
DI float swap16_sum(float x) { auto r = __builtin_amdgcn_permlane16_swap(__float_as_uint(x), __float_as_uint(x), false, false); return __uint_as_float(r[0]) + __uint_as_float(r[1]); }
DI float max32(float x) {
    x = fmaxf(x, dpp<0xB1>(x)); x = fmaxf(x, dpp<0x4E>(x)); x = fmaxf(x, dpp<0x141>(x)); x = fmaxf(x, dpp<0x140>(x)); return swap16_max(x); }
DI float sum8(float x) { x += dpp<0xB1>(x); x += dpp<0x4E>(x); x += dpp<0x141>(x); return x; }
DI float sum64(float x) { x = sum8(x); x += dpp<0x140>(x); x = swap16_sum(x); return swap32_sum(x); }

#define STAMP(i) do { } while (0)
#define ESTAMP(i) do { } while (0)
#define WSTAMP(i) do { } while (0)
#define XST(i) do { } while (0)

#define TSDECL do { } while (0)
#define TS(i) do { } while (0)
#define TSFLUSH(base) do { } while (0)
struct Ctx {
    GAS char* ws; rsrc_t rs;
    u64 tag;
    unsigned lds0, l16;
    int lane, wid, b;
};
DI Ctx fresh(const Ctx& c0) {
    Ctx c = c0;
    int lane = __builtin_amdgcn_mbcnt_hi(~0u, __builtin_amdgcn_mbcnt_lo(~0u, 0u));
    asm volatile("" : "+v"(lane));
    c.lane = lane; c.l16 = lane * 16;
    asm volatile("" : "+s"(c.wid), "+s"(c.b), "+s"(c.lds0));
    return c;
}
DI int blk_chain(int s, int j) { return (s >> 2) + 8 * ((s & 3) * 8 + j); }
DI int blk_attn(int h, int g) { return (h & 7) + 8 * (4 * g + (h >> 3)); }
#define ROT 0
DI int rot32(int s, int j) { return ROT ? (4 * j + 8 * (s & 3)) & 31 : 0; }
DI int rot7(int s, int j) { const int t = j + 2 * (s & 3); return ROT ? (t >= 7 ? (t >= 14 ? t - 14 : t - 7) : t) : 0; }
DI int chunk7(int n, int r) { const int t = n + r; return t >= 7 ? t - 7 : t; }
DI unsigned fr32(unsigned base, int n, int rot) { return base + ((n + rot) & 31) * 1024; }

template <int N> DI void publish(const Ctx& c, int phase) {
    WAITV_BAR(N);
    if (c.wid == 0 && c.lane == 0) { u64 t = c.tag; asm volatile("" : "+s"(t)); t += (u64)phase; st8(c.rs, (u32x2){(unsigned)t, (unsigned)(t >> 32)}, 0, OFF_FLAGS + (phase * 256 + c.b) * 128); }
}
template <int PER_LANE> DI void await(const Ctx& c, int phase, int pb) {
    if (c.wid == 0) {
        u64 want = c.tag; asm volatile("" : "+s"(want)); want += (u64)phase;
        for (unsigned spins = 0; spins < (1u << 18); ++spins) {
            asm volatile("" ::: "memory");
            bool ok = true;
#pragma unroll
            for (int i = 0; i < PER_LANE; ++i) { const u32x2 f = __builtin_amdgcn_raw_buffer_load_b64(c.rs, pb * 128, OFF_FLAGS + (phase * 256 + 64 * i) * 128, 16);
                ok &= f[0] == (unsigned)want && f[1] == (unsigned)(want >> 32); }
            if (__all(ok)) break;
        }
    }
    BAR();
}

constexpr unsigned OOB = 0xF0000000u;

DI void qkv_store(const Ctx& c, const f32x16& acc, const f32x16& cs, float mu, float rstd, int kind, int pi, int l, int s, int j, int tb) {
    if (kind == 0) rstd *= 0.25f * 1.4426950408889634f;
    float z[16];
#pragma unroll
    for (int i = 0; i < 16; ++i) z[i] = (acc[i] - mu * cs[i]) * rstd;
    const int tbg = 2 * s + tb, hA = 4 * j + 2 * pi;
    const unsigned base = (kind == 0 ? OFF_Q : kind == 1 ? OFF_K : OFF_V) + l * ACT_B + (hA * 64 + tbg) * 1024;
    const unsigned vo = kind == 2 ? (c.lane & 31) * 32 + (c.lane >> 5) * 16 : c.l16;
    st16(c.rs, (u32x4){cvtpk(z[0], z[1]), cvtpk(z[2], z[3]), cvtpk(z[4], z[5]), cvtpk(z[6], z[7])}, vo, base);
    st16(c.rs, (u32x4){cvtpk(z[8], z[9]), cvtpk(z[10], z[11]), cvtpk(z[12], z[13]), cvtpk(z[14], z[15])}, vo, base + 64 * 1024);
    if (kind == 1) {
        float nA = 0.f, nB = 0.f;
#pragma unroll
        for (int i = 0; i < 8; ++i) { nA += z[i] * z[i]; nB += z[8 + i] * z[8 + i]; }
        nA = max32(swap32_sum(nA)); nB = max32(swap32_sum(nB));
        if (c.lane < 2) st4(c.rs, __float_as_uint(c.lane == 0 ? nA : nB), c.lane * 256, OFF_KM + ((l * 32 + hA) * 64 + tbg) * 4);
    }
}

DI void phase_first(Ctx& c0, const Params& p, int s, int j) {
    Ctx c = fresh(c0); const int lane = c.lane, w = c.wid, b = c.b;
    TSDECL; TS(0);
    const rsrc_t rx = mkrsrc(p.x, 2048 * 512 * 2), rq = mkrsrc(p.wqkv, 512 * 1536 * 2);
    const unsigned vX = (lane >> 3) * 1024 + (((lane & 7) ^ ((((8 * w + (lane >> 3)) & 31) >> 1) & 7)) * 16);
    const unsigned vW = (lane >> 3) * 3072 + (((lane & 7) ^ (((lane >> 4) & 1) << 2)) * 16);
    const int rot8 = ROT ? (j + 2 * (s & 3)) & 7 : 0;
#define FISSUE(ch) do { const unsigned sb_ = c.lds0 + ((ch) & 3) * 32768; const int ca_ = ((ch) + rot8) & 7; \
        dma_in(rx, vX, (64 * s + 8 * w) * 1024 + ca_ * 128, sb_ + w * 1024); \
        _Pragma("unroll") for (int i = 0; i < 3; ++i) { const int pi_ = 3 * w + i; \
            dma_in(rq, vW, (64 * ca_ + 8 * (pi_ & 7)) * 3072 + ((pi_ >> 3) * 512 + 64 * j) * 2, sb_ + 8192 + pi_ * 1024); } } while (0)
    FISSUE(0); ORDER(); FISSUE(1); FISSUE(2); FISSUE(3);
    constexpr int NT0 = 0;
    {
        u64 e; asm volatile("s_load_dwordx2 %0, %1, 0x0\n\ts_waitcnt lgkmcnt(0)" : "=s"(e) : "s"(p.ws) : "memory");
        c.tag = c0.tag = e + 1;
    }
    XST(86); TS(1);
    const int nb = w < 4 ? w : 4 + (w & 1), r = lane & 31, h2 = lane >> 5;
    unsigned xa[4];
#pragma unroll
    for (int k = 0; k < 4; ++k) xa[k] = (r >> 3) * 1024 + (r & 7) * 128 + (((2 * k + h2) ^ ((r >> 1) & 7)) * 16) + (w < 4 ? 0 : ((w >> 1) & 1) * 4096);
    unsigned wa;
    { const int g = lane >> 4, q = (lane & 15) >> 2, pp = lane & 3;
      wa = 8192 + (nb >> 1) * 8192 + (8 * (g >> 1) + q) * 128 + ((4 * (nb & 1) + 2 * (g & 1) + (pp >> 1)) ^ ((q >> 1) << 2)) * 16 + (pp & 1) * 8; }
    const bf16x8 ones = asbf((u32x4){0x3f803f80u, 0x3f803f80u, 0x3f803f80u, 0x3f803f80u});
    f32x16 acc0 = {}, acc1 = {}, cs = {};
    float sm = 0.f, sq = 0.f;
#define FSTEP(ch) do { const unsigned sb_ = c.lds0 + ((ch) & 3) * 32768; \
        if (w < 4 && (((ch) + rot8) & 7) == j) {        \
            const unsigned t_ = 32 * (w >> 1) + r, a_ = sb_ + (t_ >> 3) * 1024 + (t_ & 7) * 128 + h2 * 8; u32x2 g_[4]; \
            _Pragma("unroll") for (int m = 0; m < 4; ++m) g_[m] = *(const LAS u32x2*)(uintptr_t)(a_ + (((4 * (w & 1) + m) ^ ((r >> 1) & 7)) * 16)); \
            const unsigned o_ = OFF_XT + ((2 * s + (w >> 1)) * 32 + 4 * j + 2 * (w & 1)) * 1024; \
            st16(c.rs, (u32x4){g_[0][0], g_[0][1], g_[1][0], g_[1][1]}, c.l16, o_); st16(c.rs, (u32x4){g_[2][0], g_[2][1], g_[3][0], g_[3][1]}, c.l16, o_ + 1024); } \
        const u32x4 xr = lds16(sb_ + w * 1024 + c.l16); \
        bf16x8 wfr[4]; u32x4 x0[4], x1[4]; \
        _Pragma("unroll") for (int k = 0; k < 4; ++k) { wfr[k] = cat(ldstr(sb_ + wa + k * 2048), ldstr(sb_ + wa + k * 2048 + 512)); x0[k] = lds16(sb_ + xa[k]); if (w < 4) x1[k] = lds16(sb_ + xa[k] + 4096); } \
        _Pragma("unroll") for (int i = 0; i < 4; ++i) { const float lo = bflo(xr[i]), hi = bfhi(xr[i]); sm += lo + hi; sq += lo * lo + hi * hi; } \
        _Pragma("unroll") for (int k = 0; k < 4; ++k) { \
            acc0 = MFMA(wfr[k], asbf(x0[k]), acc0); \
            if (w < 4) acc1 = MFMA(wfr[k], asbf(x1[k]), acc1); \
            cs = MFMA(wfr[k], ones, cs); } } while (0)
    rsrc_t rw = rq; unsigned ld = 1536, col0 = 0, ks0 = 4 * w, dst = OOB, csdst = OOB, src0 = OOB;
    if (b < 48) { const int jj = b / 6, i = b % 6; col0 = (i >> 1) * 512 + 64 * jj + 32 * (i & 1); dst = OFF_WQ + (b * 32 + ks0) * 1024; csdst = OFF_CSQ + b * 128; src0 = 0; }
    else if (b < 64) { rw = mkrsrc(p.wo, 512 * 512 * 2); ld = 512; col0 = 32 * (b - 48); dst = OFF_WO + ((b - 48) * 32 + ks0) * 1024; src0 = 0; }
    else if (b < 120) { rw = mkrsrc(p.w1, 512 * 1792 * 2); ld = 1792; col0 = 32 * (b - 64); dst = OFF_W1 + ((b - 64) * 32 + ks0) * 1024; csdst = OFF_CS1 + (b - 64) * 128; src0 = 0; }
    else if (b < 176) { rw = mkrsrc(p.w2, 1792 * 512 * 2); ld = 512; const int F = 32 * (b - 120) + 4 * w; col0 = 32 * (F / 112); ks0 = F % 112; dst = OFF_W2 + F * 1024; src0 = 0; }
    const unsigned stg = c.lds0 + w * 4096;
    WAITV_BAR(12 + NT0); XST(89); TS(2); FSTEP(0);
    WAITV_BAR(8 + NT0);  XST(90); FISSUE(4); FSTEP(1);
    WAITV_BAR(8 + NT0);  XST(91); FISSUE(5); FSTEP(2);
    WAITV_BAR(8 + NT0);  XST(92); FISSUE(6); FSTEP(3);
    WAITV_BAR(8);  XST(93); TS(3); FISSUE(7); FSTEP(4);
    WAITV_BAR(8);  XST(94);
    {
        const unsigned voff = ((lane >> 2) * ld + col0) * 2 + (lane & 3) * 16;
#pragma unroll
        for (int i = 0; i < 4; ++i) dma_in(rw, voff, src0 + (ks0 + i) * 16 * ld * 2, stg + i * 1024);
    }
    FSTEP(5);
    WAITV_BAR(8);  XST(95); FSTEP(6);
    WAITV_BAR(4);  XST(96); TS(4); FSTEP(7);
#undef FISSUE
#undef FSTEP
    STAMP(4 * 1 + 2); TS(5);
    sm = sum8(sm); sq = sum8(sq);
    if ((lane & 7) == 0) {
        const float mu = sm * (1.0f / 512.0f), var = fmaxf(sq * (1.0f / 512.0f) - mu * mu, 0.f);
        *(LAS f32x2*)(uintptr_t)(c.lds0 + LDS_XCH + (8 * w + (lane >> 3)) * 8) = (f32x2){mu, __builtin_amdgcn_rsqf(var + 1e-5f)};
    }
    BAR();
    if (w < 4) {
        const f32x2 t0 = *(const LAS f32x2*)(uintptr_t)(c.lds0 + LDS_XCH + r * 8), t1 = *(const LAS f32x2*)(uintptr_t)(c.lds0 + LDS_XCH + (32 + r) * 8);
        qkv_store(c, acc0, cs, t0[0], t0[1], w >> 1, w & 1, 0, s, j, 0);
        qkv_store(c, acc1, cs, t1[0], t1[1], w >> 1, w & 1, 0, s, j, 1);
    } else {
        const int tb = (w >> 1) & 1;
        const f32x2 t0 = *(const LAS f32x2*)(uintptr_t)(c.lds0 + LDS_XCH + (32 * tb + r) * 8);
        qkv_store(c, acc0, cs, t0[0], t0[1], 2, w & 1, 0, s, j, tb);
    }
    TS(6);
    publish<0>(c, PH_QKV);
    TS(7); TSFLUSH(0);
    {
        const int g = lane >> 4, hh = g >> 1, nh = g & 1, q = (lane & 15) >> 2, pp = lane & 3;
        const unsigned ta = stg + (4 * hh + q) * 64 + (16 * nh + 4 * pp) * 2;
        f32x16 cw = {};
#pragma unroll
        for (int i = 0; i < 4; ++i) {
            const bf16x8 f = cat(ldstr(ta + i * 1024), ldstr(ta + i * 1024 + 512));
            st16(c.rs, __builtin_bit_cast(u32x4, f), c.l16, dst + i * 1024);
            cw = MFMA(f, ones, cw);
        }
        if ((lane & 31) == 0) {
#pragma unroll
            for (int m = 0; m < 4; ++m) *(LAS f32x4*)(uintptr_t)(c.lds0 + LDS_XCH + 1024 + (w * 2 + (lane >> 5)) * 64 + m * 16) = (f32x4){cw[4 * m], cw[4 * m + 1], cw[4 * m + 2], cw[4 * m + 3]};
        }
        BAR();
        if (w == 0 && lane < 32) { float a = 0.f;
#pragma unroll
            for (int ww = 0; ww < 8; ++ww) a += *(const LAS float*)(uintptr_t)(c.lds0 + LDS_XCH + 1024 + ww * 128 + lane * 4);
            st4(c.rs, __float_as_uint(a), lane * 4, csdst); }
        publish<0>(c, PH_PREP);
    }
}

DI void issue_a512(const Ctx& c, int w, unsigned a_off  , unsigned st_off, int rot) {
    const unsigned tbo = (w >> 2) * 32768, n0 = 2 * (w & 3);
    dma<1>(c.rs, c.l16, st_off + w * 1024, c.lds0 + LDS_XCH + w * 1024);
    dma<2>(c.rs, c.l16, fr32(a_off + tbo, n0, rot), c.lds0 + tbo + n0 * 1024);
    ORDER();
#pragma unroll
    for (int ch = 1; ch < 4; ++ch) dma<2>(c.rs, c.l16, fr32(a_off + tbo, n0 + 8 * ch, rot), c.lds0 + tbo + (n0 + 8 * ch) * 1024);
    ORDER();
}
struct RowStat { f32x4 u0, u1, u2, u3; };
DI void stat_read(const Ctx& c, int tb, RowStat& t) {
    const unsigned r = c.lane & 31, a = c.lds0 + LDS_XCH + (32 * tb + r) * 128, k = ((4 * (c.lane >> 5)) ^ ((r >> 1) & 7)) * 16;
    t.u0 = ldsf4(a + k); t.u1 = ldsf4(a + (k ^ 16)); t.u2 = ldsf4(a + (k ^ 32)); t.u3 = ldsf4(a + (k ^ 48));
}
DI void stat_finish(const RowStat& t, float& mu, float& rstd) {
    const float sm = swap32_sum(((t.u0[0] + t.u0[2]) + (t.u1[0] + t.u1[2])) + ((t.u2[0] + t.u2[2]) + (t.u3[0] + t.u3[2])));
    const float sq = swap32_sum(((t.u0[1] + t.u0[3]) + (t.u1[1] + t.u1[3])) + ((t.u2[1] + t.u2[3]) + (t.u3[1] + t.u3[3])));
    mu = sm * (1.0f / 512.0f);
    rstd = __builtin_amdgcn_rsqf(fmaxf(sq * (1.0f / 512.0f) - mu * mu, 0.f) + 1e-5f);
}
#define KLOOP2(ab, wf, acc0, acc1, WAIT, AFTER) do { \
        u32x4 A0[3], A1[3]; \
        WAIT(0); \
        A0[0] = lds16((ab)); A1[0] = lds16((ab) + 32768); A0[1] = lds16((ab) + 1024); A1[1] = lds16((ab) + 33792); SBAR(); \
        _Pragma("unroll") for (int ks = 0; ks < 32; ++ks) { \
            if (ks == 6) { WAIT(1); } \
            if (ks + 2 < 32) { A0[(ks + 2) % 3] = lds16((ab) + (ks + 2) * 1024); A1[(ks + 2) % 3] = lds16((ab) + 32768 + (ks + 2) * 1024); } \
            SBAR(); \
            acc0 = MFMA(asbf(wf[ks]), asbf(A0[ks % 3]), acc0); acc1 = MFMA(asbf(wf[ks]), asbf(A1[ks % 3]), acc1); SBAR(); \
            AFTER(ks); SBAR(); \
        } } while (0)
#define KLOOP1(ab, wf, acc0, WAIT, AFTER) do { \
        u32x4 A0[4]; \
        WAIT(0); \
        A0[0] = lds16((ab)); A0[1] = lds16((ab) + 1024); A0[2] = lds16((ab) + 2048); SBAR(); \
        _Pragma("unroll") for (int ks = 0; ks < 32; ++ks) { \
            if (ks == 5) { WAIT(1); } \
            if (ks + 3 < 32) A0[(ks + 3) % 4] = lds16((ab) + (ks + 3) * 1024); \
            SBAR(); \
            acc0 = MFMA(asbf(wf[ks]), asbf(A0[ks % 4]), acc0); SBAR(); \
            AFTER(ks); SBAR(); \
        } } while (0)
#define NOTHING(x) do { } while (0)
DI void load_cs(const Ctx& c, unsigned off, f32x16& cs) {
#pragma unroll
    for (int m = 0; m < 4; ++m) { const f32x4 t = __builtin_bit_cast(f32x4, ld16(c.rs, (c.lane >> 5) * 64 + m * 16, off));
        cs[4 * m] = t[0]; cs[4 * m + 1] = t[1]; cs[4 * m + 2] = t[2]; cs[4 * m + 3] = t[3]; }
}

#define QKVP 10
#define QKVPRE 22
constexpr int QKV_P = QKVP, QKV_PRE = QKVPRE;
DI unsigned wq_off(int w, int j) { return OFF_WQ + (6 * j + (w < 4 ? w : 4 + (w & 1))) * 32 * 1024; }
DI void phase_qkv1(const Ctx& c0, int s, int j, u32x4 (&wf)[32]) {
    const Ctx c = fresh(c0); const int w = c.wid; OPAQUE_S(s); OPAQUE_S(j);
    const int nb = w < 4 ? w : 4 + (w & 1);
    const unsigned wo_ = wq_off(w, j); const int rot = rot32(s, j);
    f32x16 cs; load_cs(c, OFF_CSQ + (6 * j + nb) * 128, cs);
    TSDECL; TS(0);
    await<1>(c, PH_F2, blk_chain(s, c.lane & 7));
    STAMP(4 * 6 + 1); TS(1);
    issue_a512(c, w, OFF_XT + 2 * s * 32 * 1024, OFF_ST + ST_B + s * 8192, rot);
#pragma unroll
    for (int i = QKV_PRE; i < 32; ++i) wf[i] = ld16(c.rs, c.l16, fr32(wo_, i, rot));
    const unsigned ab = c.lds0 + c.l16;
#define QKV_WAIT(n) do { WAITV_BAR(((n) == 0 ? 6 : 0) + 32 - QKV_PRE); if ((n) == 0) TS(2); else TS(3); } while (0)
    {
        const int tb = w < 4 ? 0 : (w >> 1) & 1;
        f32x16 acc = {}; float mu, rs_; RowStat st;
#define QKV_AFTER(ks) do { if ((ks) == 8) stat_read(c, tb, st); if ((ks) == 13) stat_finish(st, mu, rs_); } while (0)
        KLOOP1(ab + tb * 32768, wf, acc, QKV_WAIT, QKV_AFTER);
#undef QKV_AFTER
        qkv_store(c, acc, cs, mu, rs_, w < 4 ? w >> 1 : 2, w & 1, 1, s, j, tb);
    }
    if (w < 4) {
        f32x16 acc = {}; float mu, rs_; RowStat st;
#define QKV_AFTER(ks) do { if ((ks) == 2) stat_read(c, 1, st); if ((ks) == 7) stat_finish(st, mu, rs_); } while (0)
        KLOOP1(ab + 32768, wf, acc, NOTHING, QKV_AFTER);
#undef QKV_AFTER
        STAMP(4 * 6 + 2); TS(4);
        qkv_store(c, acc, cs, mu, rs_, w >> 1, w & 1, 1, s, j, 1);
    }
#undef QKV_WAIT
    TS(5);
    publish<0>(c, PH_QKV + PH_PER_LAYER);
    TS(7); TSFLUSH(5);
}

#define W1PRE 16
#define W1MID 12
constexpr int W1_PRE = W1PRE, W1_MID = W1MID;
DI unsigned w1_off(int w, int j) { return w < 7 ? OFF_W1 + (7 * j + w) * 32 * 1024 : OOB; }
DI void phase_attn(const Ctx& c0, int l, int lp, int s, int j, u32x4 (&wf)[32], u32x4 (&wo)[8]) {
    OPAQUE_S(lp); OPAQUE_S(l); const Ctx c = fresh(c0); const int wid = c.wid, bb = c.b; OPAQUE_S(j); OPAQUE_S(s);
    const int lane = c.lane, h = (bb & 7) + 8 * ((bb >> 3) & 3), g = bb >> 5;
    const int qp = wid & 3, kh = wid >> 2;
    TSDECL; TS(0);
    __builtin_amdgcn_s_waitcnt(waitcnt_imm(0, 15));
    for (int i = wid * 64 + lane; i < ONES_BYTES / 4; i += NT) *(LAS unsigned*)(uintptr_t)(c.lds0 + LDS_ONES + i * 4) = (i & 7) == 0 ? 0x00003f80u : 0u;
    await<1>(c, PH_QKV + lp, blk_chain(lane & 31, h >> 2));
    STAMP(4 * (2 + 5 * l) + 1); TS(1);
    const unsigned kb = OFF_K + l * ACT_B + h * 65536, vb = OFF_V + l * ACT_B + h * 65536;
    u32x4 qrA, qrB; float km;
    {
        const unsigned qo = OFF_Q + l * ACT_B + (h * 64 + 8 * g + 2 * qp) * 1024, ko = OFF_KM + (l * 32 + h) * 256, l4 = lane * 4;
        asm volatile("s_nop 4\n\tbuffer_load_dwordx4 %0, %3, %4, %5 offen sc1\n\tbuffer_load_dwordx4 %1, %3, %4, %5 offen offset:1024 sc1\n\tbuffer_load_dword %2, %6, %4, %7 offen sc1"
                     : "=&v"(qrA), "=&v"(qrB), "=&v"(km) : "v"(c.l16), "s"(c.rs), "s"(qo), "v"(l4), "s"(ko) : "memory");
    }
    {
        const unsigned sk = c.lds0 + LDS_K + kh * 32768, sv = c.lds0 + LDS_V + kh * 32768;
        const unsigned tA = 8 * g + 4 * kh + qp, i0 = 8 * g + 8 + 28 * kh;
        dma<1>(c.rs, c.l16, kb + tA * 1024, sk + qp * 1024);
        dma<1>(c.rs, c.l16, vb + tA * 1024, sv + qp * 1024);
        ORDER();
        const unsigned tB = (i0 + 2 * qp) & 63;
        dma<2>(c.rs, c.l16, kb + tB * 1024, sk + (4 + 2 * qp) * 1024);
        dma<2>(c.rs, c.l16, vb + tB * 1024, sv + (4 + 2 * qp) * 1024);
    }
    TS(6);
    asm volatile("s_waitcnt vmcnt(6)" : "+v"(qrA), "+v"(qrB), "+v"(km) :: "memory");
    TS(5);
    km = swap32_max(max32(km));
    float q2A = 0.f, q2B = 0.f;
#pragma unroll
    for (int i = 0; i < 4; ++i) { float lo = bflo(qrA[i]), hi = bfhi(qrA[i]); q2A += lo * lo + hi * hi; lo = bflo(qrB[i]); hi = bfhi(qrB[i]); q2B += lo * lo + hi * hi; }
    q2A = swap32_sum(q2A); q2B = swap32_sum(q2B);
    float mA = __builtin_amdgcn_sqrtf(q2A * km) * 1.015625f, mB = __builtin_amdgcn_sqrtf(q2B * km) * 1.015625f;
    const bf16x8 qA = asbf(qrA), qB = asbf(qrB);
    const int grp = lane >> 4, h2 = grp >> 1, isone = grp & 1, qq = (lane & 15) >> 2, pp = lane & 3;
    const int inrow = qq * 32 + (pp & 1) * 16 + (pp >> 1) * 8;
    const int vstep = isone ? 0 : ATT_UNR * 1024, vstep4 = isone ? 0 : 4096;
    for (int pass = 0;; ++pass) {
        f32x16 nmA, nmB;
#pragma unroll
        for (int i = 0; i < 16; ++i) { nmA[i] = -mA; nmB[i] = -mB; }
        asm volatile("" : "+v"(nmA), "+v"(nmB));
        unsigned kpa = c.lds0 + LDS_K + kh * 32768 + c.l16; asm volatile("" : "+v"(kpa));
        unsigned vpa = c.lds0 + (isone ? LDS_ONES + (h2 ? 0 : 128) + inrow : LDS_V + kh * 32768 + h2 * 128 + inrow); asm volatile("" : "+v"(vpa));
        f32x16 oA = {}, oB = {};
#define KRD(t) asbf(lds16(kpa + (t) * 1024))
#define VTR(off) ldstr(vpa + (off))
        WAITV_BAR(4);
        TS(2);
        bf16x8 kf = KRD(0);
        s16x4 va0 = VTR(0), va1 = VTR(256), vb0 = VTR(512), vb1 = VTR(768);
        f32x16 sA = MFMA(kf, qA, nmA), sB;
        u32x4 p0, p1;
#define EX(cc, i) do { cc[i] = __builtin_amdgcn_exp2f(cc[i]); SBAR(); } while (0)
#define CV(p, jj, cc, i) do { p[jj] = cvtpk(cc[i], cc[i + 1]); SBAR(); } while (0)
#define STREAM(cur, ocur, prv, oprv, qprv, nmprv, tl, ISA, PEND) do { \
        SBAR(); \
        EX(cur, 0); EX(cur, 1); if (PEND) CV(p1, 2, prv, 12); EX(cur, 2); if (PEND) CV(p1, 3, prv, 14); EX(cur, 3); EX(cur, 4); \
        if (PEND) { oprv = MFMA(cat(vb0, vb1), asbf(p1), oprv); asm volatile("" : "+v"(oprv)); SBAR(); } \
        if (ISA && PEND) { vb0 = VTR((tl) * 1024 + 512); vb1 = VTR((tl) * 1024 + 768); SBAR(); } \
        CV(p0, 0, cur, 0); EX(cur, 5); EX(cur, 6); CV(p0, 1, cur, 2); EX(cur, 7); EX(cur, 8); \
        prv = MFMA(kf, qprv, nmprv); asm volatile("" : "+v"(prv)); SBAR(); \
        if (ISA) { kf = KRD((tl) + 1); SBAR(); } \
        CV(p0, 2, cur, 4); EX(cur, 9); EX(cur, 10); CV(p0, 3, cur, 6); EX(cur, 11); EX(cur, 12); \
        ocur = MFMA(cat(va0, va1), asbf(p0), ocur); asm volatile("" : "+v"(ocur)); SBAR(); \
        if (!(ISA)) { va0 = VTR(((tl) + 1) * 1024); va1 = VTR(((tl) + 1) * 1024 + 256); SBAR(); } \
        CV(p1, 0, cur, 8); EX(cur, 13); EX(cur, 14); CV(p1, 1, cur, 10); EX(cur, 15); \
    } while (0)
#define PAIR(tl, PEND) do { STREAM(sA, oA, sB, oB, qB, nmB, tl, true, PEND); STREAM(sB, oB, sA, oA, qA, nmA, tl, false, true); } while (0)
#define BUMP(n, vs) do { kpa += (n) * 1024; vpa += (vs); asm volatile("" : "+v"(kpa), "+v"(vpa)); } while (0)
        PAIR(0, false);
        PAIR(1, true);
        PAIR(2, true);
        WAITV_BAR(0);
        if (pass == 0) {
            int w_ = wid, b_ = bb, l_ = l; OPAQUE_S(w_); OPAQUE_S(b_); OPAQUE_S(l_);
            const int qp_ = w_ & 3, kh_ = w_ >> 2;
            const unsigned sk = c.lds0 + LDS_K + kh_ * 32768 + (12 + 5 * qp_) * 1024, i0 = 8 * (b_ >> 5) + 16 + 28 * kh_ + 5 * qp_;
            const unsigned kb_ = OFF_K + l_ * ACT_B + ((b_ & 7) + 8 * ((b_ >> 3) & 3)) * 65536;
#pragma unroll
            for (int i = 0; i < 5; ++i) { const unsigned tC = (i0 + i) & 63;
                dma<1>(c.rs, c.l16, kb_ + tC * 1024, sk + i * 1024);
                dma<1>(c.rs, c.l16, kb_ + (OFF_V - OFF_K) + tC * 1024, sk + (LDS_V - LDS_K) + i * 1024); }
        }
        PAIR(3, true);
        BUMP(4, vstep4);
#pragma unroll
        for (int u = 0; u < ATT_UNR; ++u) PAIR(u, true);
        BUMP(ATT_UNR, vstep);
        WAITV_BAR(0);
        if (l == 0) await<4>(c, PH_PREP, lane);
        STAMP(4 * (2 + 5 * l) + 0); TS(3);
        if (l == 0) WSTAMP(64);
        int w_ = wid, j_ = j, s_ = s; OPAQUE_S(w_); OPAQUE_S(j_); OPAQUE_S(s_);
        const unsigned o1 = OFF_WO + (2 * j_ + (w_ & 1)) * 32 * 1024, o2 = w1_off(w_, j_); const int rot_ = rot32(s_, j_), q4_ = 4 * (w_ >> 1);
#define PF(i) do { if ((i) < 8) wo[(i) & 7] = ld16(c.rs, c.l16, fr32(o1, 16 * (((i) & 7) >> 2) + ((i) & 3) + q4_, rot_)); \
        else if ((i) < 8 + W1_PRE) wf[((i) - 8) & 31] = ld16(c.rs, c.l16, fr32(o2, (i) - 8, rot_)); SBAR(); } while (0)
#pragma unroll
        for (int blk = 0; blk < 3; ++blk) {
#pragma unroll
            for (int u = 0; u < ATT_UNR; ++u) {
                STREAM(sA, oA, sB, oB, qB, nmB, u, true, true);  PF(2 * (ATT_UNR * blk + u));
                STREAM(sB, oB, sA, oA, qA, nmA, u, false, true); PF(2 * (ATT_UNR * blk + u) + 1);
            }
            BUMP(ATT_UNR, vstep);
        }
#undef PF
        CV(p1, 2, sB, 12); CV(p1, 3, sB, 14);
        oB = MFMA(cat(vb0, vb1), asbf(p1), oB);
        STAMP(4 * (2 + 5 * l) + 2); TS(4);
        if (l == 0) WSTAMP(56);
        {
            const f32x16 give = kh ? oA : oB;
            const unsigned xb = c.lds0 + LDS_XCH + wid * 2304;
            *(LAS f32x4*)(uintptr_t)(xb + c.l16) = (f32x4){give[0], give[1], give[2], give[3]};
            *(LAS f32x4*)(uintptr_t)(xb + 1024 + c.l16) = (f32x4){give[4], give[5], give[6], give[7]};
            *(LAS float*)(uintptr_t)(xb + 2048 + lane * 4) = give[8];
            if (wid == 0 && lane == 0) *(LAS unsigned*)(uintptr_t)(c.lds0 + LDS_FLAG) = 0u;
        }
        BAR();
        f32x16 o = kh ? oB : oA;
        {
            const unsigned xb = c.lds0 + LDS_XCH + (wid ^ 4) * 2304;
            const f32x4 u0 = ldsf4(xb + c.l16), u1 = ldsf4(xb + 1024 + c.l16);
            const float ul = *(const LAS float*)(uintptr_t)(xb + 2048 + lane * 4);
#pragma unroll
            for (int i = 0; i < 4; ++i) { o[i] += u0[i]; o[4 + i] += u1[i]; }
            o[8] = swap32_lo(o[8] + ul);
        }
        const float rl = __builtin_amdgcn_rcpf(o[8]);
        int w2_ = wid, b2_ = bb, l2_ = l; OPAQUE_S(w2_); OPAQUE_S(b2_); OPAQUE_S(l2_);
        const unsigned o_off = OFF_O + l2_ * ACT_B + ((8 * (b2_ >> 5) + 2 * (w2_ & 3) + (w2_ >> 2)) * 32 + (b2_ & 7) + 8 * ((b2_ >> 3) & 3)) * 1024;
        st16(c.rs, (u32x4){cvtpk(o[0] * rl, o[1] * rl), cvtpk(o[2] * rl, o[3] * rl), cvtpk(o[4] * rl, o[5] * rl), cvtpk(o[6] * rl, o[7] * rl)}, c.l16, o_off);
        const bool bad = !(o[8] >= 8.673617379884035e-19f   && o[8] <= 3.0e38f);
        if (pass == 0 && __any(bad) && lane == 0) *(LAS unsigned*)(uintptr_t)(c.lds0 + LDS_FLAG) = 1u;
        WAITV_BAR(0);
        TS(7);
        if (pass || __builtin_amdgcn_readfirstlane(*(const LAS unsigned*)(uintptr_t)(c.lds0 + LDS_FLAG)) == 0u) break;
        mA = -3.0e38f; mB = -3.0e38f;
        const f32x16 zero = {};
        for (int t = 0; t < 64; ++t) {
            const bf16x8 kk = asbf(lds16(c.lds0 + LDS_K + t * 1024 + c.l16));
            const f32x16 a = MFMA(kk, qA, zero), bq = MFMA(kk, qB, zero);
#pragma unroll
            for (int i = 0; i < 16; ++i) { mA = fmaxf(mA, a[i]); mB = fmaxf(mB, bq[i]); }
        }
        mA = swap32_max(mA); mB = swap32_max(mB);
        BAR();
#undef KRD
#undef VTR
#undef EX
#undef CV
#undef STREAM
#undef PAIR
#undef BUMP
    }
    TSFLUSH(l == 0 ? 1 : 6);
    if (c.wid == 0 && c.lane == 0) { u64 t = c.tag; asm volatile("" : "+s"(t)); t += (u64)(PH_ATT + lp);
        st8(c.rs, (u32x2){(unsigned)t, (unsigned)(t >> 32)}, 0, OFF_FLAGS + ((PH_ATT + lp) * 256 + c.b) * 128); }
    asm volatile("" ::: "memory");
    {
        int w_ = wid, j_ = j, s_ = s; OPAQUE_S(w_); OPAQUE_S(j_); OPAQUE_S(s_);
        const unsigned o2 = w1_off(w_, j_); const int rot_ = rot32(s_, j_);
#pragma unroll
        for (int i = W1_PRE; i < W1_PRE + W1_MID; ++i) wf[i] = ld16(c.rs, c.l16, fr32(o2, i, rot_));
    }
}

DI void res_finish(const Ctx& c, const Params& p, int w, int s, int j, bool last, const f32x16& v, unsigned x_off, unsigned st_off, unsigned (&xres)[8]) {
    const int lane = c.lane, tb = w >> 1, nb = w & 1;
    f32x2 y[8];
#pragma unroll
    for (int m = 0; m < 8; ++m) { y[m] = (f32x2){bflo(xres[m]), bfhi(xres[m])} + (f32x2){v[2 * m], v[2 * m + 1]}; xres[m] = cvtpk(y[m][0], y[m][1]); }
    if (last) {
        const rsrc_t ro = mkrsrc(p.out, 2048 * 512 * 2);
        const unsigned vo = (64 * s + 32 * tb + (lane & 31)) * 1024 + (64 * j + 32 * nb) * 2 + (lane >> 5) * 16;
#pragma unroll
        for (int k = 0; k < 2; ++k) {
            const auto a = __builtin_amdgcn_permlane32_swap(xres[4 * k], xres[4 * k + 2], false, false);
            const auto b = __builtin_amdgcn_permlane32_swap(xres[4 * k + 1], xres[4 * k + 3], false, false);
            __builtin_amdgcn_raw_buffer_store_b128((u32x4){a[0], b[0], a[1], b[1]}, ro, vo + 32 * k, 0, 0);
        }
        return;
    }
    const unsigned base = x_off + ((2 * s + tb) * 32 + 4 * j + 2 * nb) * 1024;
    st16(c.rs, (u32x4){xres[0], xres[1], xres[2], xres[3]}, c.l16, base);
    st16(c.rs, (u32x4){xres[4], xres[5], xres[6], xres[7]}, c.l16, base + 1024);
    f32x2 s2 = (y[0] + y[1]) + (y[2] + y[3]), q2 = y[0] * y[0];
    s2 += (y[4] + y[5]) + (y[6] + y[7]);
#pragma unroll
    for (int m = 1; m < 8; ++m) q2 += y[m] * y[m];
    const float sm = swap32_sum(s2[0] + s2[1]), sq = swap32_sum(q2[0] + q2[1]);
    if (lane < 32) st8(c.rs, (u32x2){__float_as_uint(sm), __float_as_uint(sq)}, lane * 128, st_off + (64 * s + 32 * tb) * 128 + (2 * j + nb) * 8);
}
DI void put16(unsigned a, const f32x16& v) {
#pragma unroll
    for (int m = 0; m < 4; ++m) *(LAS f32x4*)(uintptr_t)(a + m * 1024) = (f32x4){v[4 * m], v[4 * m + 1], v[4 * m + 2], v[4 * m + 3]};
}
DI void add16x3(unsigned a, f32x16& v) {
    f32x4 t[12];
#pragma unroll
    for (int m = 0; m < 12; ++m) t[m] = ldsf4(a + m * 1024);
#pragma unroll
    for (int m = 0; m < 4; ++m) { const f32x4 u = (t[m] + t[4 + m]) + t[8 + m]; v[4 * m] += u[0]; v[4 * m + 1] += u[1]; v[4 * m + 2] += u[2]; v[4 * m + 3] += u[3]; }
}

DI void parts_out(unsigned base, int nb, int q, int tbA, const f32x16& acc0, const f32x16& acc1) {
    const int sl = q > 0 ? q - 1 : 0;
    put16(base + ((nb + 2 * (1 - tbA)) * 3 + sl) * 4096, acc1);
    if (q >= 2) put16(base + (nb * 3 + sl) * 4096, acc0);
}
DI void phase_wo(const Ctx& c0, const Params& p, int l, int lp, int s, int j, unsigned (&xres)[8], u32x4 (&wo)[8], u32x4 (&wf)[32]) {
    OPAQUE_S(lp); OPAQUE_S(l); const Ctx c = fresh(c0); const int w = c.wid; OPAQUE_S(s); OPAQUE_S(j);
    const int nb = w & 1, q = w >> 1, tbA = q == 1 ? 1 : 0;
    if (w < 4) {
        const unsigned o = OFF_XT + ((2 * s + (w >> 1)) * 32 + 4 * j + 2 * nb) * 1024;
        const u32x4 a = ld16(c.rs, c.l16, o), b = ld16(c.rs, c.l16, o + 1024);
#pragma unroll
        for (int m = 0; m < 4; ++m) { xres[m] = a[m]; xres[4 + m] = b[m]; }
    }
    TSDECL; TS(0);
    await<1>(c, PH_ATT + lp, blk_attn(c.lane & 31, s >> 2));
    STAMP(4 * (3 + 5 * l) + 1); TS(1);
    const unsigned a_off = OFF_O + l * ACT_B + 2 * s * 32 * 1024;
    const int rot = rot32(s, j);
#define ISSUE(n) do { const unsigned o_ = 16 * ((n) >> 1) + 4 * (w & 3) + 2 * ((n) & 1); \
        dma<2>(c.rs, c.l16, fr32(a_off + (w >> 2) * 32768, o_, rot), c.lds0 + (w >> 2) * 32768 + o_ * 1024); } while (0)
    ISSUE(0); ORDER(); ISSUE(1); ISSUE(2); ISSUE(3); ORDER();
#undef ISSUE
    const unsigned nxt = w1_off(w, j);
    f32x16 acc0 = {}, acc1 = {};
    const unsigned ab0 = c.lds0 + c.l16 + q * 4096 + tbA * 32768, ab1 = c.lds0 + c.l16 + q * 4096 + (1 - tbA) * 32768;
#define COMPUTE(n) do { const unsigned o_ = (16 * ((n) >> 1) + 2 * ((n) & 1)) * 1024; u32x4 A0[2], A1[2]; \
        _Pragma("unroll") for (int i = 0; i < 2; ++i) { A0[i] = lds16(ab0 + o_ + i * 1024); A1[i] = lds16(ab1 + o_ + i * 1024); } SBAR(); \
        _Pragma("unroll") for (int i = 0; i < 2; ++i) { \
            acc0 = MFMA(asbf(wo[2 * (n) + i]), asbf(A0[i]), acc0); acc1 = MFMA(asbf(wo[2 * (n) + i]), asbf(A1[i]), acc1); SBAR(); } } while (0)
    WAITV_BAR(6); TS(2); COMPUTE(0);
    WAITV_BAR(0); COMPUTE(1); COMPUTE(2); COMPUTE(3);
#undef COMPUTE
    STAMP(4 * (3 + 5 * l) + 2); TS(3);
    parts_out(c.lds0 + 65536 + c.l16, nb, q, tbA, acc0, acc1);
    BAR();
    TS(4);
    if (w < 4) {
        const unsigned xa = c.lds0 + 65536 + w * 3 * 4096 + c.l16;
        add16x3(xa, acc0);
        res_finish(c, p, w, s, j, false, acc0, OFF_X1 + l * ACT_B, OFF_ST + (2 * l) * ST_B, xres);
    }
    TS(5);
    publish<0>(c, PH_WO + lp);
    TS(6);
#pragma unroll
    for (int i = W1_PRE + W1_MID; i < 32; ++i) wf[i] = ld16(c.rs, c.l16, fr32(nxt, i, rot));
    TS(7); if (l == 0) TSFLUSH(2);
}

#define W2MID 14
constexpr int W2_MID = W2MID;
DI unsigned w2_off(int w, int j) { return OFF_W2 + ((2 * j + (w & 1)) * 112 + 4 * (w >> 1)) * 1024; }
DI void phase_f1(const Ctx& c0, int l, int lp, int s, int j, u32x4 (&wf)[32]) {
    OPAQUE_S(lp); OPAQUE_S(l); const Ctx c = fresh(c0); const int w = c.wid; OPAQUE_S(s); OPAQUE_S(j);
    const int nb = 7 * j + w;
    f32x16 cs; load_cs(c, w < 7 ? OFF_CS1 + nb * 128 : OOB, cs);
    TSDECL; TS(0);
    await<1>(c, PH_WO + lp, blk_chain(s, c.lane & 7));
    TS(1);
    STAMP(4 * (4 + 5 * l) + 1);
    if (l == 0) XST(79);
    const int rot = rot32(s, j), r7 = rot7(s, j);
    issue_a512(c, w, OFF_X1 + l * ACT_B + 2 * s * 32 * 1024, OFF_ST + (2 * l) * ST_B + s * 8192, rot);
    constexpr int LATE1 = TRAIL ? 0 : 32 - W1_PRE - W1_MID;
    const unsigned ab = c.lds0 + c.l16, nxt = w2_off(w, j);
#define W2FRAG(i) ld16(c.rs, c.l16, nxt + (16 * chunk7((i) >> 2, r7) + ((i) & 3)) * 1024)
#define F1_WAIT(n) do { WAITV_BAR(((n) == 0 ? 6 : 0) + LATE1); if (l == 0) XST(80 + (n)); if ((n) == 0) TS(2); if ((n) == 1) TS(3); } while (0)
#define F1_OUT(tb) do { float z[16]; \
        _Pragma("unroll") for (int i = 0; i < 16; ++i) z[i] = (acc[i] - mu * cs[i]) * rs_; \
        const unsigned base = w < 7 ? OFF_H + l * H_B + ((2 * s + (tb)) * 112 + 2 * nb) * 1024 : OOB; \
        st16(c.rs, (u32x4){relupk(z[0], z[1]), relupk(z[2], z[3]), relupk(z[4], z[5]), relupk(z[6], z[7])}, c.l16, base); \
        st16(c.rs, (u32x4){relupk(z[8], z[9]), relupk(z[10], z[11]), relupk(z[12], z[13]), relupk(z[14], z[15])}, c.l16, base + 1024); } while (0)
    {
        f32x16 acc = {}; float mu, rs_; RowStat st;
#define F1_AFTER(ks) do { if ((ks) == 8) stat_read(c, 0, st); if ((ks) == 13) stat_finish(st, mu, rs_); } while (0)
        KLOOP1(ab, wf, acc, F1_WAIT, F1_AFTER);
#undef F1_AFTER
        F1_OUT(0);
    }
    {
        f32x16 acc = {}; float mu, rs_; RowStat st;
#define F1_AFTER(ks) do { if ((ks) < W2_MID) wf[ks] = W2FRAG(ks); if ((ks) == 22) stat_read(c, 1, st); if ((ks) == 27) stat_finish(st, mu, rs_); } while (0)
        KLOOP1(ab + 32768, wf, acc, NOTHING, F1_AFTER);
#undef F1_AFTER
        TS(4);
        STAMP(4 * (4 + 5 * l) + 2);
        F1_OUT(1);
    }
#undef F1_WAIT
#undef F1_OUT
    TS(5);
    publish<0>(c, PH_F1 + lp);
    TS(6);
#pragma unroll
    for (int i = W2_MID; i < 28; ++i) wf[i] = W2FRAG(i);
#undef W2FRAG
    TS(7); if (l == 0) TSFLUSH(3);
}

DI void phase_f2(const Ctx& c0, const Params& p, int l, int lp, int s, int j, unsigned (&xres)[8], u32x4 (&wf)[32]) {
    OPAQUE_S(lp); OPAQUE_S(l); const Ctx c = fresh(c0); const int w = c.wid; OPAQUE_S(s); OPAQUE_S(j);
    const int nb = w & 1, q = w >> 1, tbA = q == 1 ? 1 : 0;
    const bool last = l != 0;
    TSDECL; TS(0);
    await<1>(c, PH_F1 + lp, blk_chain(s, c.lane & 7));
    STAMP(4 * (5 + 5 * l) + 1); TS(1);
    const unsigned a_off = OFF_H + l * H_B + 2 * s * 112 * 1024;
    const int r7 = rot7(s, j), rot = rot32(s, j);
#define ISSUE(ch) dma<4>(c.rs, c.l16, a_off + ((w >> 2) * 112 + 16 * chunk7(ch, r7) + 4 * (w & 3)) * 1024, c.lds0 + ((ch) & 3) * 32768 + (w >> 2) * 16384 + 4 * (w & 3) * 1024)
    f32x16 acc0 = {}, acc1 = {};
    const unsigned ab0 = c.lds0 + c.l16 + q * 4096 + tbA * 16384, ab1 = c.lds0 + c.l16 + q * 4096 + (1 - tbA) * 16384;
#define COMPUTE(ch) do { const unsigned o_ = ((ch) & 3) * 32768; u32x4 A0[4], A1[4]; \
        _Pragma("unroll") for (int i = 0; i < 2; ++i) { A0[i] = lds16(ab0 + o_ + i * 1024); A1[i] = lds16(ab1 + o_ + i * 1024); } SBAR(); \
        _Pragma("unroll") for (int i = 0; i < 4; ++i) { \
            if (i < 2) { A0[i + 2] = lds16(ab0 + o_ + (i + 2) * 1024); A1[i + 2] = lds16(ab1 + o_ + (i + 2) * 1024); } SBAR(); \
            acc0 = MFMA(asbf(wf[4 * (ch) + i]), asbf(A0[i]), acc0); acc1 = MFMA(asbf(wf[4 * (ch) + i]), asbf(A1[i]), acc1); SBAR(); } } while (0)
    const unsigned nxt = wq_off(w, j);
    constexpr int P = QKV_P;
    ISSUE(0); ORDER(); ISSUE(1); ORDER(); ISSUE(2); ISSUE(3); ORDER();
    constexpr int R = TRAIL ? 0 : 28 - W2_MID;
    WAITV_BAR(12 + R); TS(2); COMPUTE(0);
    WAITV_BAR(8 + R); ISSUE(4); COMPUTE(1);
    WAITV_BAR(4 + R + 4); ISSUE(5); COMPUTE(2);
    WAITV_BAR(R + 8); ISSUE(6); COMPUTE(3);
    WAITV_BAR(8); COMPUTE(4);
    WAITV_BAR(4); COMPUTE(5);
    WAITV_BAR(0); TS(3); COMPUTE(6);
#undef ISSUE
#undef COMPUTE
    STAMP(4 * (5 + 5 * l) + 2);
    parts_out(c.lds0 + c.l16, nb, q, tbA, acc0, acc1);
    BAR();
    TS(4);
    if (w < 4) {
        const unsigned xa = c.lds0 + w * 3 * 4096 + c.l16;
        add16x3(xa, acc0);
        res_finish(c, p, w, s, j, last, acc0, OFF_XT, OFF_ST + ST_B, xres);
    }
    if (last) { __builtin_amdgcn_s_waitcnt(waitcnt_imm(0, 15)); return; }
    asm volatile("" ::: "memory");
#pragma unroll
    for (int i = 0; i < P; ++i) wf[i] = ld16(c.rs, c.l16, fr32(nxt, i, rot));
    TS(5);
    publish<P>(c, PH_F2);
    TS(6);
#pragma unroll
    for (int i = P; i < QKV_PRE; ++i) wf[i] = ld16(c.rs, c.l16, fr32(nxt, i, rot));
    TS(7); TSFLUSH(4);
}

__global__ void __launch_bounds__(NT, 2) fused_layers_kernel(Params p) {
    extern __shared__ __attribute__((aligned(16))) char lds[];
    Ctx c;
    c.ws = (GAS char*)p.ws; c.rs = mkrsrc(p.ws, WS_BYTES);
    c.lds0 = (unsigned)(uintptr_t)(LAS char*)lds;
    c.lane = threadIdx.x & 63; c.wid = __builtin_amdgcn_readfirstlane(threadIdx.x >> 6); c.l16 = c.lane * 16; c.b = blockIdx.x;
    c.tag = 0;
    const int x = c.b & 7, r = c.b >> 3, s = 4 * x + (r >> 3), j = r & 7;
    {
        u64 pc; asm volatile("s_getpc_b64 %0" : "=s"(pc));
        if (c.wid == 7) warm4k(mkrsrc((const void*)(pc & ~(u64)63), 37 * 1024), r < 10 ? r * 4096 + c.lane * 64 : OOB, 0, c.lds0 + LDS_DUMMY);
    }
    unsigned xres[8];
    u32x4 wf[32], wo[8];
    ESTAMP(0);
    phase_first(c, p, s, j);
    ESTAMP(7);
#pragma unroll 1
    for (int l_ = 0;; ++l_) {
        int l = __builtin_amdgcn_readfirstlane(l_); OPAQUE_S(l);
        int lp = PH_PER_LAYER * l; OPAQUE_S(lp);
        phase_attn(c, l, lp, s, j, wf, wo);
        ESTAMP(4 * (2 + 5 * l) + 3);
        phase_wo(c, p, l, lp, s, j, xres, wo, wf);
        ESTAMP(4 * (3 + 5 * l) + 3);
        phase_f1(c, l, lp, s, j, wf);
        ESTAMP(4 * (4 + 5 * l) + 3);
        phase_f2(c, p, l, lp, s, j, xres, wf);
        ESTAMP(4 * (5 + 5 * l) + 3);
        if (l != 0) break;
        phase_qkv1(c, s, j, wf);
        ESTAMP(4 * 6 + 3);
    }
    const Ctx ce = fresh(c);
    if (ce.b == 0 && ce.wid == 0 && ce.lane == 0) { const u64 t = ce.tag + 15; st8(ce.rs, (u32x2){(unsigned)t, (unsigned)(t >> 32)}, 0, OFF_EPOCH); }
}

}

extern "C" void kernel_launch(void* const* d_in, const int* in_sizes, int n_in, void* d_out, int out_size, void* d_ws, size_t ws_size, hipStream_t stream) {
    (void)in_sizes; (void)n_in; (void)out_size; (void)ws_size;
    Params p;
    p.x = d_in[0]; p.wqkv = d_in[1]; p.wo = d_in[2]; p.w1 = d_in[4]; p.w2 = d_in[5]; p.out = d_out; p.ws = d_ws;
    (void)hipFuncSetAttribute((const void*)fused_layers_kernel, hipFuncAttributeMaxDynamicSharedMemorySize, LDS_TOTAL);
    hipLaunchKernelGGL(fused_layers_kernel, dim3(256), dim3(NT), LDS_TOTAL, stream, p);
}
```

```cpp
#include <hip/hip_runtime.h>
#include <hip/hip_bf16.h>
#include <stdint.h>

namespace {

#define GAS __attribute__((address_space(1)))
#define LAS __attribute__((address_space(3)))
#define DI __device__ __forceinline__
typedef __bf16 bf16x8 __attribute__((ext_vector_type(8)));
typedef __bf16 bf16x2 __attribute__((ext_vector_type(2)));
typedef short s16x4 __attribute__((ext_vector_type(4)));
typedef float f32x2 __attribute__((ext_vector_type(2)));
typedef float f32x4 __attribute__((ext_vector_type(4)));
typedef float f32x16 __attribute__((ext_vector_type(16)));
typedef unsigned u32x2 __attribute__((ext_vector_type(2)));
typedef unsigned u32x4 __attribute__((ext_vector_type(4)));
typedef unsigned long long u64;
typedef __amdgpu_buffer_rsrc_t rsrc_t;

#define P0TOUCH 0
#define TRAIL 1
#define WARM 0
constexpr int NT = 512;
constexpr unsigned OFF_EPOCH = 0;
constexpr unsigned OFF_FLAGS = 0x1000;
constexpr unsigned OFF_PROF  = 0x2D00000;
constexpr unsigned OFF_CSQ   = 0x70000;
constexpr unsigned OFF_CS1   = 0x72000;
constexpr unsigned OFF_KM    = 0x74000;
constexpr unsigned OFF_ST    = 0x2C00000;
constexpr unsigned OFF_WQ    = 0x100000;
constexpr unsigned OFF_WO    = 0x280000;
constexpr unsigned OFF_W1    = 0x300000;
constexpr unsigned OFF_W2    = 0x4C0000;
constexpr unsigned OFF_XT    = 0x700000;
constexpr unsigned OFF_Q     = 0xA00000;
constexpr unsigned OFF_K     = 0xE00000;
constexpr unsigned OFF_V     = 0x1200000;
constexpr unsigned OFF_O     = 0x1600000;
constexpr unsigned OFF_X1    = 0x1A00000;
constexpr unsigned OFF_H     = 0x1E00000;
constexpr unsigned WS_BYTES  = 0x2E80000;
constexpr unsigned ACT_B = 0x200000, H_B = 0x700000, ST_B = 0x40000;
constexpr int PH_PREP = 0, PH_QKV = 1, PH_ATT = 2, PH_WO = 3, PH_F1 = 4, PH_F2 = 5, PH_PER_LAYER = 5;
constexpr int ATT_UNR = 7;
constexpr int LDS_K = 0, LDS_V = 65536, LDS_ONES = 131072, ONES_BYTES = (ATT_UNR + 2) * 1024 + 256;
constexpr int LDS_XCH = LDS_ONES + ONES_BYTES;
constexpr int LDS_FLAG = LDS_XCH + 8 * 2304;
constexpr int LDS_DUMMY = LDS_FLAG + 64;
constexpr int LDS_TOTAL = LDS_DUMMY + 4096;
static_assert(LDS_TOTAL <= 163840, "LDS");

struct Params { const void* x; const void* wqkv; const void* wo; const void* w1; const void* w2; void* out; void* ws; };

#define MFMA(a, b, c) __builtin_amdgcn_mfma_f32_32x32x16_bf16((a), (b), (c), 0, 0, 0)
#define SBAR() __builtin_amdgcn_sched_barrier(0)
#define BAR() asm volatile("s_waitcnt lgkmcnt(0)\n\ts_barrier" ::: "memory")
constexpr int waitcnt_imm(int vm, int lgkm) { return (vm & 15) | ((vm >> 4) << 14) | (7 << 4) | (lgkm << 8); }
#define WAITV_BAR(n) do { asm volatile("" ::: "memory"); __builtin_amdgcn_s_waitcnt(waitcnt_imm(n, 0)); asm volatile("s_barrier" ::: "memory"); } while (0)
#define ORDER() asm volatile("s_barrier" ::: "memory")
#define DRAIN() asm volatile("s_waitcnt vmcnt(0)" ::: "memory")

DI unsigned cvtpk(float lo, float hi) { f32x2 v = {lo, hi}; return __builtin_bit_cast(unsigned, __builtin_convertvector(v, bf16x2)); }
DI unsigned relupk(float lo, float hi) { typedef short s16x2 __attribute__((ext_vector_type(2)));
    const s16x2 v = __builtin_bit_cast(s16x2, cvtpk(lo, hi)); return __builtin_bit_cast(unsigned, __builtin_elementwise_max(v, (s16x2){0, 0})); }
DI bf16x8 asbf(u32x4 v) { return __builtin_bit_cast(bf16x8, v); }
DI float bflo(unsigned u) { return __uint_as_float(u << 16); }
DI float bfhi(unsigned u) { return __uint_as_float(u & 0xffff0000u); }
DI rsrc_t mkrsrc(const void* p, unsigned bytes) { return __builtin_amdgcn_make_buffer_rsrc((void*)p, 0, bytes, 0x00020000); }
DI u32x4 ld16(rsrc_t rs, unsigned voff, unsigned soff) { return __builtin_amdgcn_raw_buffer_load_b128(rs, voff, soff, 16); }
DI unsigned ld4(rsrc_t rs, unsigned voff, unsigned soff) { return __builtin_amdgcn_raw_buffer_load_b32(rs, voff, soff, 16); }
DI void st16(rsrc_t rs, u32x4 v, unsigned voff, unsigned soff) { __builtin_amdgcn_raw_buffer_store_b128(v, rs, voff + soff, 0, 16); }
DI void st8(rsrc_t rs, u32x2 v, unsigned voff, unsigned soff) { __builtin_amdgcn_raw_buffer_store_b64(v, rs, voff, soff, 16); }
DI void st4(rsrc_t rs, unsigned v, unsigned voff, unsigned soff) { __builtin_amdgcn_raw_buffer_store_b32(v, rs, voff, soff, 16); }
#define DMA_HEAD "s_nop 2\n\ts_mov_b32 m0, %0\n\ts_nop 0\n\t"
#define DMA_ONE(off, pol) "buffer_load_dwordx4 %1, %2, %3 offen offset:" #off pol " lds\n\t"
template <int N> DI void dma(rsrc_t rs, unsigned voff, unsigned soff, unsigned ldsdst) {
    if constexpr (N == 1) asm volatile(DMA_HEAD DMA_ONE(0, " sc1") :: "s"(ldsdst), "v"(voff), "s"(rs), "s"(soff) : "memory");
    if constexpr (N == 2) asm volatile(DMA_HEAD DMA_ONE(0, " sc1") DMA_ONE(1024, " sc1") :: "s"(ldsdst), "v"(voff), "s"(rs), "s"(soff) : "memory");
    if constexpr (N == 3) asm volatile(DMA_HEAD DMA_ONE(0, " sc1") DMA_ONE(1024, " sc1") DMA_ONE(2048, " sc1") :: "s"(ldsdst), "v"(voff), "s"(rs), "s"(soff) : "memory");
    if constexpr (N == 4) asm volatile(DMA_HEAD DMA_ONE(0, " sc1") DMA_ONE(1024, " sc1") DMA_ONE(2048, " sc1") DMA_ONE(3072, " sc1") :: "s"(ldsdst), "v"(voff), "s"(rs), "s"(soff) : "memory");
}
DI void dma_in(rsrc_t rs, unsigned voff, unsigned soff, unsigned ldsdst) {
    asm volatile(DMA_HEAD DMA_ONE(0, "") :: "s"(ldsdst), "v"(voff), "s"(rs), "s"(soff) : "memory");
}
DI void warm4k(rsrc_t rs, unsigned l64, unsigned soff, unsigned ldsdst) {
    asm volatile("s_nop 2\n\ts_mov_b32 m0, %0\n\ts_nop 0\n\tbuffer_load_dword %1, %2, %3 offen lds" :: "s"(ldsdst), "v"(l64), "s"(rs), "s"(soff) : "memory");
}
#define OPAQUE_S(x) asm volatile("" : "+s"(x))
#define PIN4(a, i) asm volatile("" : "+v"(a[i]), "+v"(a[(i) + 1]), "+v"(a[(i) + 2]), "+v"(a[(i) + 3]))
DI u32x4 lds16(unsigned a) { return *(const LAS u32x4*)(uintptr_t)a; }
DI f32x4 ldsf4(unsigned a) { return *(const LAS f32x4*)(uintptr_t)a; }
DI s16x4 ldstr(unsigned a) { return __builtin_amdgcn_ds_read_tr16_b64_v4i16((LAS s16x4*)(uintptr_t)a); }
DI bf16x8 cat(s16x4 a, s16x4 b) { return __builtin_bit_cast(bf16x8, __builtin_shufflevector(a, b, 0, 1, 2, 3, 4, 5, 6, 7)); }
template <int CTRL> DI float dpp(float x) { return __builtin_bit_cast(float, __builtin_amdgcn_mov_dpp(__builtin_bit_cast(int, x), CTRL, 0xf, 0xf, true)); }
DI float swap32_sum(float x) { auto r = __builtin_amdgcn_permlane32_swap(__float_as_uint(x), __float_as_uint(x), false, false); return __uint_as_float(r[0]) + __uint_as_float(r[1]); }
DI float swap32_max(float x) { auto r = __builtin_amdgcn_permlane32_swap(__float_as_uint(x), __float_as_uint(x), false, false); return fmaxf(__uint_as_float(r[0]), __uint_as_float(r[1])); }
DI float swap32_lo(float x) { auto r = __builtin_amdgcn_permlane32_swap(__float_as_uint(x), __float_as_uint(x), false, false); return __uint_as_float(r[0]); }
DI float swap16_max(float x) { auto r = __builtin_amdgcn_permlane16_swap(__float_as_uint(x), __float_as_uint(x), false, false); return fmaxf(__uint_as_float(r[0]), __uint_as_float(r[1])); }
DI float swap16_sum(float x) { auto r = __builtin_amdgcn_permlane16_swap(__float_as_uint(x), __float_as_uint(x), false, false); return __uint_as_float(r[0]) + __uint_as_float(r[1]); }
DI float max32(float x) {
    x = fmaxf(x, dpp<0xB1>(x)); x = fmaxf(x, dpp<0x4E>(x)); x = fmaxf(x, dpp<0x141>(x)); x = fmaxf(x, dpp<0x140>(x)); return swap16_max(x); }
DI float sum8(float x) { x += dpp<0xB1>(x); x += dpp<0x4E>(x); x += dpp<0x141>(x); return x; }
DI float sum64(float x) { x = sum8(x); x += dpp<0x140>(x); x = swap16_sum(x); return swap32_sum(x); }

#define STAMP(i) do { } while (0)
#define ESTAMP(i) do { } while (0)
#define WSTAMP(i) do { } while (0)
#define XST(i) do { } while (0)

#define TSDECL do { } while (0)
#define TS(i) do { } while (0)
#define TSFLUSH(base) do { } while (0)
struct Ctx {
    GAS char* ws; rsrc_t rs;
    u64 tag;
    unsigned lds0, l16;
    int lane, wid, b;
};
DI Ctx fresh(const Ctx& c0) {
    Ctx c = c0;
    int lane = __builtin_amdgcn_mbcnt_hi(~0u, __builtin_amdgcn_mbcnt_lo(~0u, 0u));
    asm volatile("" : "+v"(lane));
    c.lane = lane; c.l16 = lane * 16;
    asm volatile("" : "+s"(c.wid), "+s"(c.b), "+s"(c.lds0));
    return c;
}
DI int blk_chain(int s, int j) { return (s >> 2) + 8 * ((s & 3) * 8 + j); }
DI int blk_attn(int h, int g) { return (h & 7) + 8 * (4 * g + (h >> 3)); }
#define ROT 0
DI int rot32(int s, int j) { return ROT ? (4 * j + 8 * (s & 3)) & 31 : 0; }
DI int rot7(int s, int j) { const int t = j + 2 * (s & 3); return ROT ? (t >= 7 ? (t >= 14 ? t - 14 : t - 7) : t) : 0; }
DI int chunk7(int n, int r) { const int t = n + r; return t >= 7 ? t - 7 : t; }
DI unsigned fr32(unsigned base, int n, int rot) { return base + ((n + rot) & 31) * 1024; }

template <int N> DI void publish(const Ctx& c, int phase) {
    WAITV_BAR(N);
    if (c.wid == 0 && c.lane == 0) { u64 t = c.tag; asm volatile("" : "+s"(t)); t += (u64)phase; st8(c.rs, (u32x2){(unsigned)t, (unsigned)(t >> 32)}, 0, OFF_FLAGS + (phase * 256 + c.b) * 128); }
}
template <int PER_LANE> DI void await(const Ctx& c, int phase, int pb) {
    if (c.wid == 0) {
        u64 want = c.tag; asm volatile("" : "+s"(want)); want += (u64)phase;
        for (unsigned spins = 0; spins < (1u << 18); ++spins) {
            asm volatile("" ::: "memory");
            bool ok = true;
#pragma unroll
            for (int i = 0; i < PER_LANE; ++i) { const u32x2 f = __builtin_amdgcn_raw_buffer_load_b64(c.rs, pb * 128, OFF_FLAGS + (phase * 256 + 64 * i) * 128, 16);
                ok &= f[0] == (unsigned)want && f[1] == (unsigned)(want >> 32); }
            if (__all(ok)) break;
        }
    }
    BAR();
}

constexpr unsigned OOB = 0xF0000000u;

DI void qkv_store(const Ctx& c, const f32x16& acc, const f32x16& cs, float mu, float rstd, int kind, int pi, int l, int s, int j, int tb) {
    if (kind == 0) rstd *= 0.25f * 1.4426950408889634f;
    float z[16];
#pragma unroll
    for (int i = 0; i < 16; ++i) z[i] = (acc[i] - mu * cs[i]) * rstd;
    const int tbg = 2 * s + tb, hA = 4 * j + 2 * pi;
    const unsigned base = (kind == 0 ? OFF_Q : kind == 1 ? OFF_K : OFF_V) + l * ACT_B + (hA * 64 + tbg) * 1024;
    const unsigned vo = kind == 2 ? (c.lane & 31) * 32 + (c.lane >> 5) * 16 : c.l16;
    st16(c.rs, (u32x4){cvtpk(z[0], z[1]), cvtpk(z[2], z[3]), cvtpk(z[4], z[5]), cvtpk(z[6], z[7])}, vo, base);
    st16(c.rs, (u32x4){cvtpk(z[8], z[9]), cvtpk(z[10], z[11]), cvtpk(z[12], z[13]), cvtpk(z[14], z[15])}, vo, base + 64 * 1024);
    if (kind == 1) {
        float nA = 0.f, nB = 0.f;
#pragma unroll
        for (int i = 0; i < 8; ++i) { nA += z[i] * z[i]; nB += z[8 + i] * z[8 + i]; }
        nA = max32(swap32_sum(nA)); nB = max32(swap32_sum(nB));
        if (c.lane < 2) st4(c.rs, __float_as_uint(c.lane == 0 ? nA : nB), c.lane * 256, OFF_KM + ((l * 32 + hA) * 64 + tbg) * 4);
    }
}

DI void phase_first(Ctx& c0, const Params& p, int s, int j) {
    Ctx c = fresh(c0); const int lane = c.lane, w = c.wid, b = c.b;
    TSDECL; TS(0);
    const rsrc_t rx = mkrsrc(p.x, 2048 * 512 * 2), rq = mkrsrc(p.wqkv, 512 * 1536 * 2);
    const unsigned vX = (lane >> 3) * 1024 + (((lane & 7) ^ ((((8 * w + (lane >> 3)) & 31) >> 1) & 7)) * 16);
    const unsigned vW = (lane >> 3) * 3072 + (((lane & 7) ^ (((lane >> 4) & 1) << 2)) * 16);
    const int rot8 = ROT ? (j + 2 * (s & 3)) & 7 : 0;
#define FISSUE(ch) do { const unsigned sb_ = c.lds0 + ((ch) & 3) * 32768; const int ca_ = ((ch) + rot8) & 7; \
        dma_in(rx, vX, (64 * s + 8 * w) * 1024 + ca_ * 128, sb_ + w * 1024); \
        _Pragma("unroll") for (int i = 0; i < 3; ++i) { const int pi_ = 3 * w + i; \
            dma_in(rq, vW, (64 * ca_ + 8 * (pi_ & 7)) * 3072 + ((pi_ >> 3) * 512 + 64 * j) * 2, sb_ + 8192 + pi_ * 1024); } } while (0)
    FISSUE(0); ORDER(); FISSUE(1); FISSUE(2); FISSUE(3);
    constexpr int NT0 = 0;
    {
        u64 e; asm volatile("s_load_dwordx2 %0, %1, 0x0\n\ts_waitcnt lgkmcnt(0)" : "=s"(e) : "s"(p.ws) : "memory");
        c.tag = c0.tag = e + 1;
    }
    XST(86); TS(1);
    const int nb = w < 4 ? w : 4 + (w & 1), r = lane & 31, h2 = lane >> 5;
    unsigned xa[4];
#pragma unroll
    for (int k = 0; k < 4; ++k) xa[k] = (r >> 3) * 1024 + (r & 7) * 128 + (((2 * k + h2) ^ ((r >> 1) & 7)) * 16) + (w < 4 ? 0 : ((w >> 1) & 1) * 4096);
    unsigned wa;
    { const int g = lane >> 4, q = (lane & 15) >> 2, pp = lane & 3;
      wa = 8192 + (nb >> 1) * 8192 + (8 * (g >> 1) + q) * 128 + ((4 * (nb & 1) + 2 * (g & 1) + (pp >> 1)) ^ ((q >> 1) << 2)) * 16 + (pp & 1) * 8; }
    const bf16x8 ones = asbf((u32x4){0x3f803f80u, 0x3f803f80u, 0x3f803f80u, 0x3f803f80u});
    f32x16 acc0 = {}, acc1 = {}, cs = {};
    float sm = 0.f, sq = 0.f;
    u32x2 xg[4] = {};
#define FSTEP(ch) do { const unsigned sb_ = c.lds0 + ((ch) & 3) * 32768; \
        if (w < 4 && (((ch) + rot8) & 7) == j) {        \
            const unsigned t_ = 32 * (w >> 1) + r, a_ = sb_ + (t_ >> 3) * 1024 + (t_ & 7) * 128 + h2 * 8; \
            _Pragma("unroll") for (int m = 0; m < 4; ++m) xg[m] = *(const LAS u32x2*)(uintptr_t)(a_ + (((4 * (w & 1) + m) ^ ((r >> 1) & 7)) * 16)); } \
        const u32x4 xr = lds16(sb_ + w * 1024 + c.l16); \
        bf16x8 wfr[4]; u32x4 x0[4], x1[4]; \
        _Pragma("unroll") for (int k = 0; k < 4; ++k) { wfr[k] = cat(ldstr(sb_ + wa + k * 2048), ldstr(sb_ + wa + k * 2048 + 512)); x0[k] = lds16(sb_ + xa[k]); if (w < 4) x1[k] = lds16(sb_ + xa[k] + 4096); } \
        _Pragma("unroll") for (int i = 0; i < 4; ++i) { const float lo = bflo(xr[i]), hi = bfhi(xr[i]); sm += lo + hi; sq += lo * lo + hi * hi; } \
        _Pragma("unroll") for (int k = 0; k < 4; ++k) { \
            acc0 = MFMA(wfr[k], asbf(x0[k]), acc0); \
            if (w < 4) acc1 = MFMA(wfr[k], asbf(x1[k]), acc1); \
            cs = MFMA(wfr[k], ones, cs); } } while (0)
    rsrc_t rw = rq; unsigned ld = 1536, col0 = 0, ks0 = 4 * w, dst = OOB, csdst = OOB, src0 = OOB;
    if (b < 48) { const int jj = b / 6, i = b % 6; col0 = (i >> 1) * 512 + 64 * jj + 32 * (i & 1); dst = OFF_WQ + (b * 32 + ks0) * 1024; csdst = OFF_CSQ + b * 128; src0 = 0; }
    else if (b < 64) { rw = mkrsrc(p.wo, 512 * 512 * 2); ld = 512; col0 = 32 * (b - 48); dst = OFF_WO + ((b - 48) * 32 + ks0) * 1024; src0 = 0; }
    else if (b < 120) { rw = mkrsrc(p.w1, 512 * 1792 * 2); ld = 1792; col0 = 32 * (b - 64); dst = OFF_W1 + ((b - 64) * 32 + ks0) * 1024; csdst = OFF_CS1 + (b - 64) * 128; src0 = 0; }
    else if (b < 176) { rw = mkrsrc(p.w2, 1792 * 512 * 2); ld = 512; const int F = 32 * (b - 120) + 4 * w; col0 = 32 * (F / 112); ks0 = F % 112; dst = OFF_W2 + F * 1024; src0 = 0; }
    const unsigned stg = c.lds0 + w * 4096;
    WAITV_BAR(12 + NT0); XST(89); TS(2); FSTEP(0);
    WAITV_BAR(8 + NT0);  XST(90); FISSUE(4); FSTEP(1);
    WAITV_BAR(8 + NT0);  XST(91); FISSUE(5); FSTEP(2);
    WAITV_BAR(8 + NT0);  XST(92); FISSUE(6); FSTEP(3);
    WAITV_BAR(8);  XST(93); TS(3); FISSUE(7); FSTEP(4);
    WAITV_BAR(8);  XST(94);
    {
        const unsigned voff = ((lane >> 2) * ld + col0) * 2 + (lane & 3) * 16;
#pragma unroll
        for (int i = 0; i < 4; ++i) dma_in(rw, voff, src0 + (ks0 + i) * 16 * ld * 2, stg + i * 1024);
    }
    FSTEP(5);
    WAITV_BAR(8);  XST(95); FSTEP(6);
    WAITV_BAR(4);  XST(96); TS(4); FSTEP(7);
#undef FISSUE
#undef FSTEP
    STAMP(4 * 1 + 2); TS(5);
    sm = sum8(sm); sq = sum8(sq);
    if ((lane & 7) == 0) {
        const float mu = sm * (1.0f / 512.0f), var = fmaxf(sq * (1.0f / 512.0f) - mu * mu, 0.f);
        *(LAS f32x2*)(uintptr_t)(c.lds0 + LDS_XCH + (8 * w + (lane >> 3)) * 8) = (f32x2){mu, __builtin_amdgcn_rsqf(var + 1e-5f)};
    }
    BAR();
    if (w < 4) {
        const unsigned o_ = OFF_XT + ((2 * s + (w >> 1)) * 32 + 4 * j + 2 * (w & 1)) * 1024;
        st16(c.rs, (u32x4){xg[0][0], xg[0][1], xg[1][0], xg[1][1]}, c.l16, o_); st16(c.rs, (u32x4){xg[2][0], xg[2][1], xg[3][0], xg[3][1]}, c.l16, o_ + 1024);
        const f32x2 t0 = *(const LAS f32x2*)(uintptr_t)(c.lds0 + LDS_XCH + r * 8), t1 = *(const LAS f32x2*)(uintptr_t)(c.lds0 + LDS_XCH + (32 + r) * 8);
        qkv_store(c, acc0, cs, t0[0], t0[1], w >> 1, w & 1, 0, s, j, 0);
        qkv_store(c, acc1, cs, t1[0], t1[1], w >> 1, w & 1, 0, s, j, 1);
    } else {
        const int tb = (w >> 1) & 1;
        const f32x2 t0 = *(const LAS f32x2*)(uintptr_t)(c.lds0 + LDS_XCH + (32 * tb + r) * 8);
        qkv_store(c, acc0, cs, t0[0], t0[1], 2, w & 1, 0, s, j, tb);
    }
    TS(6);
    publish<0>(c, PH_QKV);
    TS(7); TSFLUSH(0);
    {
        const int g = lane >> 4, hh = g >> 1, nh = g & 1, q = (lane & 15) >> 2, pp = lane & 3;
        const unsigned ta = stg + (4 * hh + q) * 64 + (16 * nh + 4 * pp) * 2;
        f32x16 cw = {};
#pragma unroll
        for (int i = 0; i < 4; ++i) {
            const bf16x8 f = cat(ldstr(ta + i * 1024), ldstr(ta + i * 1024 + 512));
            st16(c.rs, __builtin_bit_cast(u32x4, f), c.l16, dst + i * 1024);
            cw = MFMA(f, ones, cw);
        }
        if ((lane & 31) == 0) {
#pragma unroll
            for (int m = 0; m < 4; ++m) *(LAS f32x4*)(uintptr_t)(c.lds0 + LDS_XCH + 1024 + (w * 2 + (lane >> 5)) * 64 + m * 16) = (f32x4){cw[4 * m], cw[4 * m + 1], cw[4 * m + 2], cw[4 * m + 3]};
        }
        BAR();
        if (w == 0 && lane < 32) { float a = 0.f;
#pragma unroll
            for (int ww = 0; ww < 8; ++ww) a += *(const LAS float*)(uintptr_t)(c.lds0 + LDS_XCH + 1024 + ww * 128 + lane * 4);
            st4(c.rs, __float_as_uint(a), lane * 4, csdst); }
        publish<0>(c, PH_PREP);
    }
}

DI void issue_a512(const Ctx& c, int w, unsigned a_off  , unsigned st_off, int rot) {
    const unsigned tbo = (w >> 2) * 32768, n0 = 2 * (w & 3);
    dma<1>(c.rs, c.l16, st_off + w * 1024, c.lds0 + LDS_XCH + w * 1024);
    dma<2>(c.rs, c.l16, fr32(a_off + tbo, n0, rot), c.lds0 + tbo + n0 * 1024);
    ORDER();
#pragma unroll
    for (int ch = 1; ch < 4; ++ch) dma<2>(c.rs, c.l16, fr32(a_off + tbo, n0 + 8 * ch, rot), c.lds0 + tbo + (n0 + 8 * ch) * 1024);
    ORDER();
}
struct RowStat { f32x4 u0, u1, u2, u3; };
DI void stat_read(const Ctx& c, int tb, RowStat& t) {
    const unsigned r = c.lane & 31, a = c.lds0 + LDS_XCH + (32 * tb + r) * 128, k = ((4 * (c.lane >> 5)) ^ ((r >> 1) & 7)) * 16;
    t.u0 = ldsf4(a + k); t.u1 = ldsf4(a + (k ^ 16)); t.u2 = ldsf4(a + (k ^ 32)); t.u3 = ldsf4(a + (k ^ 48));
}
DI void stat_finish(const RowStat& t, float& mu, float& rstd) {
    const float sm = swap32_sum(((t.u0[0] + t.u0[2]) + (t.u1[0] + t.u1[2])) + ((t.u2[0] + t.u2[2]) + (t.u3[0] + t.u3[2])));
    const float sq = swap32_sum(((t.u0[1] + t.u0[3]) + (t.u1[1] + t.u1[3])) + ((t.u2[1] + t.u2[3]) + (t.u3[1] + t.u3[3])));
    mu = sm * (1.0f / 512.0f);
    rstd = __builtin_amdgcn_rsqf(fmaxf(sq * (1.0f / 512.0f) - mu * mu, 0.f) + 1e-5f);
}
#define KLOOP2(ab, wf, acc0, acc1, WAIT, AFTER) do { \
        u32x4 A0[3], A1[3]; \
        WAIT(0); \
        A0[0] = lds16((ab)); A1[0] = lds16((ab) + 32768); A0[1] = lds16((ab) + 1024); A1[1] = lds16((ab) + 33792); SBAR(); \
        _Pragma("unroll") for (int ks = 0; ks < 32; ++ks) { \
            if (ks == 6) { WAIT(1); } \
            if (ks + 2 < 32) { A0[(ks + 2) % 3] = lds16((ab) + (ks + 2) * 1024); A1[(ks + 2) % 3] = lds16((ab) + 32768 + (ks + 2) * 1024); } \
            SBAR(); \
            acc0 = MFMA(asbf(wf[ks]), asbf(A0[ks % 3]), acc0); acc1 = MFMA(asbf(wf[ks]), asbf(A1[ks % 3]), acc1); SBAR(); \
            AFTER(ks); SBAR(); \
        } } while (0)
#define KLOOP1(ab, wf, acc0, WAIT, AFTER) do { \
        u32x4 A0[4]; \
        WAIT(0); \
        A0[0] = lds16((ab)); A0[1] = lds16((ab) + 1024); A0[2] = lds16((ab) + 2048); SBAR(); \
        _Pragma("unroll") for (int ks = 0; ks < 32; ++ks) { \
            if (ks == 5) { WAIT(1); } \
            if (ks + 3 < 32) A0[(ks + 3) % 4] = lds16((ab) + (ks + 3) * 1024); \
            SBAR(); \
            acc0 = MFMA(asbf(wf[ks]), asbf(A0[ks % 4]), acc0); SBAR(); \
            AFTER(ks); SBAR(); \
        } } while (0)
#define NOTHING(x) do { } while (0)
DI void load_cs(const Ctx& c, unsigned off, f32x16& cs) {
#pragma unroll
    for (int m = 0; m < 4; ++m) { const f32x4 t = __builtin_bit_cast(f32x4, ld16(c.rs, (c.lane >> 5) * 64 + m * 16, off));
        cs[4 * m] = t[0]; cs[4 * m + 1] = t[1]; cs[4 * m + 2] = t[2]; cs[4 * m + 3] = t[3]; }
}

#define QKVP 10
#define QKVPRE 16
constexpr int QKV_P = QKVP, QKV_PRE = QKVPRE;
DI unsigned wq_off(int w, int j) { return OFF_WQ + (6 * j + (w < 4 ? w : 4 + (w & 1))) * 32 * 1024; }
DI void phase_qkv1(const Ctx& c0, int s, int j, u32x4 (&wf)[32]) {
    const Ctx c = fresh(c0); const int w = c.wid; OPAQUE_S(s); OPAQUE_S(j);
    const int nb = w < 4 ? w : 4 + (w & 1);
    const unsigned wo_ = wq_off(w, j); const int rot = rot32(s, j);
    f32x16 cs; load_cs(c, OFF_CSQ + (6 * j + nb) * 128, cs);
    TSDECL; TS(0);
    await<1>(c, PH_F2, blk_chain(s, c.lane & 7));
    STAMP(4 * 6 + 1); TS(1);
    issue_a512(c, w, OFF_XT + 2 * s * 32 * 1024, OFF_ST + ST_B + s * 8192, rot);
    const unsigned ab = c.lds0 + c.l16;
#define QKV_WAIT(n) do { WAITV_BAR((n) == 0 ? 6 : 5); if ((n) == 0) TS(2); else TS(3); } while (0)
#define QKV_LATE(ks) do { if (QKV_PRE + (ks) < 32) wf[(QKV_PRE + (ks)) & 31] = ld16(c.rs, c.l16, fr32(wo_, QKV_PRE + (ks), rot)); } while (0)
    {
        const int tb = w < 4 ? 0 : (w >> 1) & 1;
        f32x16 acc = {}; float mu, rs_; RowStat st;
#define QKV_AFTER(ks) do { QKV_LATE(ks); if ((ks) == 8) stat_read(c, tb, st); if ((ks) == 13) stat_finish(st, mu, rs_); } while (0)
        KLOOP1(ab + tb * 32768, wf, acc, QKV_WAIT, QKV_AFTER);
#undef QKV_AFTER
        qkv_store(c, acc, cs, mu, rs_, w < 4 ? w >> 1 : 2, w & 1, 1, s, j, tb);
    }
    if (w < 4) {
        f32x16 acc = {}; float mu, rs_; RowStat st;
#define QKV_AFTER(ks) do { if ((ks) == 2) stat_read(c, 1, st); if ((ks) == 7) stat_finish(st, mu, rs_); } while (0)
        KLOOP1(ab + 32768, wf, acc, NOTHING, QKV_AFTER);
#undef QKV_AFTER
        STAMP(4 * 6 + 2); TS(4);
        qkv_store(c, acc, cs, mu, rs_, w >> 1, w & 1, 1, s, j, 1);
    }
#undef QKV_WAIT
#undef QKV_LATE
    TS(5);
    publish<0>(c, PH_QKV + PH_PER_LAYER);
    TS(7); TSFLUSH(5);
}

#define ATT_PRIO 1
#define ATT_PRIO2 0
#define ATT_PRIO_END 18
#define W1PRE 16
#define W1MID 12
constexpr int W1_PRE = W1PRE, W1_MID = W1MID;
DI unsigned w1_off(int w, int j) { return w < 7 ? OFF_W1 + (7 * j + w) * 32 * 1024 : OOB; }
DI void phase_attn(const Ctx& c0, int l, int lp, int s, int j, u32x4 (&wf)[32], u32x4 (&wo)[8]) {
    OPAQUE_S(lp); OPAQUE_S(l); const Ctx c = fresh(c0); const int wid = c.wid, bb = c.b; OPAQUE_S(j); OPAQUE_S(s);
    const int lane = c.lane, h = (bb & 7) + 8 * ((bb >> 3) & 3), g = bb >> 5;
    const int qp = wid & 3, kh = wid >> 2;
    TSDECL; TS(0);
    __builtin_amdgcn_s_waitcnt(waitcnt_imm(0, 15));
    for (int i = wid * 64 + lane; i < ONES_BYTES / 4; i += NT) *(LAS unsigned*)(uintptr_t)(c.lds0 + LDS_ONES + i * 4) = (i & 7) == 0 ? 0x00003f80u : 0u;
    await<1>(c, PH_QKV + lp, blk_chain(lane & 31, h >> 2));
    STAMP(4 * (2 + 5 * l) + 1); TS(1);
    const unsigned kb = OFF_K + l * ACT_B + h * 65536, vb = OFF_V + l * ACT_B + h * 65536;
    u32x4 qrA, qrB; float km;
    {
        const unsigned qo = OFF_Q + l * ACT_B + (h * 64 + 8 * g + 2 * qp) * 1024, ko = OFF_KM + (l * 32 + h) * 256, l4 = lane * 4;
        asm volatile("s_nop 4\n\tbuffer_load_dwordx4 %0, %3, %4, %5 offen sc1\n\tbuffer_load_dwordx4 %1, %3, %4, %5 offen offset:1024 sc1\n\tbuffer_load_dword %2, %6, %4, %7 offen sc1"
                     : "=&v"(qrA), "=&v"(qrB), "=&v"(km) : "v"(c.l16), "s"(c.rs), "s"(qo), "v"(l4), "s"(ko) : "memory");
    }
    {
        const unsigned sk = c.lds0 + LDS_K + kh * 32768, sv = c.lds0 + LDS_V + kh * 32768;
        const unsigned tA = 8 * g + 4 * kh + qp, i0 = 8 * g + 8 + 28 * kh;
        dma<1>(c.rs, c.l16, kb + tA * 1024, sk + qp * 1024);
        dma<1>(c.rs, c.l16, vb + tA * 1024, sv + qp * 1024);
        ORDER();
        const unsigned tB = (i0 + 2 * qp) & 63;
        dma<2>(c.rs, c.l16, kb + tB * 1024, sk + (4 + 2 * qp) * 1024);
        dma<2>(c.rs, c.l16, vb + tB * 1024, sv + (4 + 2 * qp) * 1024);
    }
    TS(6);
    asm volatile("s_waitcnt vmcnt(6)" : "+v"(qrA), "+v"(qrB), "+v"(km) :: "memory");
    TS(5);
    km = swap32_max(max32(km));
    float q2A = 0.f, q2B = 0.f;
#pragma unroll
    for (int i = 0; i < 4; ++i) { float lo = bflo(qrA[i]), hi = bfhi(qrA[i]); q2A += lo * lo + hi * hi; lo = bflo(qrB[i]); hi = bfhi(qrB[i]); q2B += lo * lo + hi * hi; }
    q2A = swap32_sum(q2A); q2B = swap32_sum(q2B);
    float mA = __builtin_amdgcn_sqrtf(q2A * km) * 1.015625f, mB = __builtin_amdgcn_sqrtf(q2B * km) * 1.015625f;
    const bf16x8 qA = asbf(qrA), qB = asbf(qrB);
    const int grp = lane >> 4, h2 = grp >> 1, isone = grp & 1, qq = (lane & 15) >> 2, pp = lane & 3;
    const int inrow = qq * 32 + (pp & 1) * 16 + (pp >> 1) * 8;
    const int vstep = isone ? 0 : ATT_UNR * 1024, vstep4 = isone ? 0 : 4096;
    for (int pass = 0;; ++pass) {
        f32x16 nmA, nmB;
#pragma unroll
        for (int i = 0; i < 16; ++i) { nmA[i] = -mA; nmB[i] = -mB; }
        asm volatile("" : "+v"(nmA), "+v"(nmB));
        unsigned kpa = c.lds0 + LDS_K + kh * 32768 + c.l16; asm volatile("" : "+v"(kpa));
        unsigned vpa = c.lds0 + (isone ? LDS_ONES + (h2 ? 0 : 128) + inrow : LDS_V + kh * 32768 + h2 * 128 + inrow); asm volatile("" : "+v"(vpa));
        f32x16 oA = {}, oB = {};
#define KRD(t) asbf(lds16(kpa + (t) * 1024))
#define VTR(off) ldstr(vpa + (off))
        WAITV_BAR(4);
        TS(2);
        bf16x8 kf = KRD(0);
        s16x4 va0 = VTR(0), va1 = VTR(256), vb0 = VTR(512), vb1 = VTR(768);
        f32x16 sA = MFMA(kf, qA, nmA), sB;
        u32x4 p0, p1;
#define EX(cc, i) do { cc[i] = __builtin_amdgcn_exp2f(cc[i]); SBAR(); } while (0)
#define CV(p, jj, cc, i) do { p[jj] = cvtpk(cc[i], cc[i + 1]); SBAR(); } while (0)
#define STREAM(cur, ocur, prv, oprv, qprv, nmprv, tl, ISA, PEND) do { \
        SBAR(); \
        EX(cur, 0); EX(cur, 1); if (PEND) CV(p1, 2, prv, 12); EX(cur, 2); if (PEND) CV(p1, 3, prv, 14); EX(cur, 3); EX(cur, 4); \
        if (PEND) { oprv = MFMA(cat(vb0, vb1), asbf(p1), oprv); asm volatile("" : "+v"(oprv)); SBAR(); } \
        if (ISA && PEND) { vb0 = VTR((tl) * 1024 + 512); vb1 = VTR((tl) * 1024 + 768); SBAR(); } \
        CV(p0, 0, cur, 0); EX(cur, 5); EX(cur, 6); CV(p0, 1, cur, 2); EX(cur, 7); EX(cur, 8); \
        prv = MFMA(kf, qprv, nmprv); asm volatile("" : "+v"(prv)); SBAR(); \
        if (ISA) { kf = KRD((tl) + 1); SBAR(); } \
        CV(p0, 2, cur, 4); EX(cur, 9); EX(cur, 10); CV(p0, 3, cur, 6); EX(cur, 11); EX(cur, 12); \
        ocur = MFMA(cat(va0, va1), asbf(p0), ocur); asm volatile("" : "+v"(ocur)); SBAR(); \
        if (!(ISA)) { va0 = VTR(((tl) + 1) * 1024); va1 = VTR(((tl) + 1) * 1024 + 256); SBAR(); } \
        CV(p1, 0, cur, 8); EX(cur, 13); EX(cur, 14); CV(p1, 1, cur, 10); EX(cur, 15); \
    } while (0)
#define PAIR(tl, PEND) do { STREAM(sA, oA, sB, oB, qB, nmB, tl, true, PEND); STREAM(sB, oB, sA, oA, qA, nmA, tl, false, true); } while (0)
#define BUMP(n, vs) do { kpa += (n) * 1024; vpa += (vs); asm volatile("" : "+v"(kpa), "+v"(vpa)); } while (0)
        PAIR(0, false);
        PAIR(1, true);
        PAIR(2, true);
        WAITV_BAR(0);
        if (pass == 0) {
            int w_ = wid, b_ = bb, l_ = l; OPAQUE_S(w_); OPAQUE_S(b_); OPAQUE_S(l_);
            const int qp_ = w_ & 3, kh_ = w_ >> 2;
            const unsigned sk = c.lds0 + LDS_K + kh_ * 32768 + (12 + 5 * qp_) * 1024, i0 = 8 * (b_ >> 5) + 16 + 28 * kh_ + 5 * qp_;
            const unsigned kb_ = OFF_K + l_ * ACT_B + ((b_ & 7) + 8 * ((b_ >> 3) & 3)) * 65536;
#pragma unroll
            for (int i = 0; i < 5; ++i) { const unsigned tC = (i0 + i) & 63;
                dma<1>(c.rs, c.l16, kb_ + tC * 1024, sk + i * 1024);
                dma<1>(c.rs, c.l16, kb_ + (OFF_V - OFF_K) + tC * 1024, sk + (LDS_V - LDS_K) + i * 1024); }
        }
        PAIR(3, true);
        BUMP(4, vstep4);
#pragma unroll
        for (int u = 0; u < ATT_UNR; ++u) {
            PAIR(u, true); }
        BUMP(ATT_UNR, vstep);
        WAITV_BAR(0);
        if (l == 0) await<4>(c, PH_PREP, lane);
        STAMP(4 * (2 + 5 * l) + 0); TS(3);
        if (l == 0) WSTAMP(64);
        int w_ = wid, j_ = j, s_ = s; OPAQUE_S(w_); OPAQUE_S(j_); OPAQUE_S(s_);
        const unsigned o1 = OFF_WO + (2 * j_ + (w_ & 1)) * 32 * 1024, o2 = w1_off(w_, j_); const int rot_ = rot32(s_, j_), q4_ = 4 * (w_ >> 1);
#define PF(i) do { if ((i) < 8) wo[(i) & 7] = ld16(c.rs, c.l16, fr32(o1, 16 * (((i) & 7) >> 2) + ((i) & 3) + q4_, rot_)); \
        else if ((i) < 8 + W1_PRE) wf[((i) - 8) & 31] = ld16(c.rs, c.l16, fr32(o2, (i) - 8, rot_)); SBAR(); } while (0)
        if (w_ >= 4) __builtin_amdgcn_s_setprio(1);
#pragma unroll
        for (int blk = 0; blk < 3; ++blk) {
#pragma unroll
            for (int u = 0; u < ATT_UNR; ++u) {
                if (ATT_UNR * blk + u == ATT_PRIO_END) __builtin_amdgcn_s_setprio(0);
                STREAM(sA, oA, sB, oB, qB, nmB, u, true, true);  PF(2 * (ATT_UNR * blk + u));
                STREAM(sB, oB, sA, oA, qA, nmA, u, false, true); PF(2 * (ATT_UNR * blk + u) + 1);
            }
            BUMP(ATT_UNR, vstep);
        }
#undef PF
        CV(p1, 2, sB, 12); CV(p1, 3, sB, 14);
        oB = MFMA(cat(vb0, vb1), asbf(p1), oB);
        STAMP(4 * (2 + 5 * l) + 2); TS(4);
        if (l == 0) WSTAMP(56);
        {
            const f32x16 give = kh ? oA : oB;
            const unsigned xb = c.lds0 + LDS_XCH + wid * 2304;
            *(LAS f32x4*)(uintptr_t)(xb + c.l16) = (f32x4){give[0], give[1], give[2], give[3]};
            *(LAS f32x4*)(uintptr_t)(xb + 1024 + c.l16) = (f32x4){give[4], give[5], give[6], give[7]};
            *(LAS float*)(uintptr_t)(xb + 2048 + lane * 4) = give[8];
            if (wid == 0 && lane == 0) *(LAS unsigned*)(uintptr_t)(c.lds0 + LDS_FLAG) = 0u;
        }
        BAR();
        f32x16 o = kh ? oB : oA;
        {
            const unsigned xb = c.lds0 + LDS_XCH + (wid ^ 4) * 2304;
            const f32x4 u0 = ldsf4(xb + c.l16), u1 = ldsf4(xb + 1024 + c.l16);
            const float ul = *(const LAS float*)(uintptr_t)(xb + 2048 + lane * 4);
#pragma unroll
            for (int i = 0; i < 4; ++i) { o[i] += u0[i]; o[4 + i] += u1[i]; }
            o[8] = swap32_lo(o[8] + ul);
        }
        const float rl = __builtin_amdgcn_rcpf(o[8]);
        int w2_ = wid, b2_ = bb, l2_ = l; OPAQUE_S(w2_); OPAQUE_S(b2_); OPAQUE_S(l2_);
        const unsigned o_off = OFF_O + l2_ * ACT_B + ((8 * (b2_ >> 5) + 2 * (w2_ & 3) + (w2_ >> 2)) * 32 + (b2_ & 7) + 8 * ((b2_ >> 3) & 3)) * 1024;
        st16(c.rs, (u32x4){cvtpk(o[0] * rl, o[1] * rl), cvtpk(o[2] * rl, o[3] * rl), cvtpk(o[4] * rl, o[5] * rl), cvtpk(o[6] * rl, o[7] * rl)}, c.l16, o_off);
        const bool bad = !(o[8] >= 8.673617379884035e-19f   && o[8] <= 3.0e38f);
        if (pass == 0 && __any(bad) && lane == 0) *(LAS unsigned*)(uintptr_t)(c.lds0 + LDS_FLAG) = 1u;
        WAITV_BAR(0);
        TS(7);
        if (pass || __builtin_amdgcn_readfirstlane(*(const LAS unsigned*)(uintptr_t)(c.lds0 + LDS_FLAG)) == 0u) break;
        mA = -3.0e38f; mB = -3.0e38f;
        const f32x16 zero = {};
        for (int t = 0; t < 64; ++t) {
            const bf16x8 kk = asbf(lds16(c.lds0 + LDS_K + t * 1024 + c.l16));
            const f32x16 a = MFMA(kk, qA, zero), bq = MFMA(kk, qB, zero);
#pragma unroll
            for (int i = 0; i < 16; ++i) { mA = fmaxf(mA, a[i]); mB = fmaxf(mB, bq[i]); }
        }
        mA = swap32_max(mA); mB = swap32_max(mB);
        BAR();
#undef KRD
#undef VTR
#undef EX
#undef CV
#undef STREAM
#undef PAIR
#undef BUMP
    }
    TSFLUSH(l == 0 ? 1 : 6);
    if (c.wid == 0 && c.lane == 0) { u64 t = c.tag; asm volatile("" : "+s"(t)); t += (u64)(PH_ATT + lp);
        st8(c.rs, (u32x2){(unsigned)t, (unsigned)(t >> 32)}, 0, OFF_FLAGS + ((PH_ATT + lp) * 256 + c.b) * 128); }
    asm volatile("" ::: "memory");
    {
        int w_ = wid, j_ = j, s_ = s; OPAQUE_S(w_); OPAQUE_S(j_); OPAQUE_S(s_);
        const unsigned o2 = w1_off(w_, j_); const int rot_ = rot32(s_, j_);
#pragma unroll
        for (int i = W1_PRE; i < W1_PRE + W1_MID; ++i) wf[i] = ld16(c.rs, c.l16, fr32(o2, i, rot_));
    }
}

DI void res_finish(const Ctx& c, const Params& p, int w, int s, int j, bool last, const f32x16& v, unsigned x_off, unsigned st_off, unsigned (&xres)[8]) {
    const int lane = c.lane, tb = w >> 1, nb = w & 1;
    f32x2 y[8];
#pragma unroll
    for (int m = 0; m < 8; ++m) { y[m] = (f32x2){bflo(xres[m]), bfhi(xres[m])} + (f32x2){v[2 * m], v[2 * m + 1]}; xres[m] = cvtpk(y[m][0], y[m][1]); }
    if (last) {
        const rsrc_t ro = mkrsrc(p.out, 2048 * 512 * 2);
        const unsigned vo = (64 * s + 32 * tb + (lane & 31)) * 1024 + (64 * j + 32 * nb) * 2 + (lane >> 5) * 16;
#pragma unroll
        for (int k = 0; k < 2; ++k) {
            const auto a = __builtin_amdgcn_permlane32_swap(xres[4 * k], xres[4 * k + 2], false, false);
            const auto b = __builtin_amdgcn_permlane32_swap(xres[4 * k + 1], xres[4 * k + 3], false, false);
            __builtin_amdgcn_raw_buffer_store_b128((u32x4){a[0], b[0], a[1], b[1]}, ro, vo + 32 * k, 0, 0);
        }
        return;
    }
    const unsigned base = x_off + ((2 * s + tb) * 32 + 4 * j + 2 * nb) * 1024;
    st16(c.rs, (u32x4){xres[0], xres[1], xres[2], xres[3]}, c.l16, base);
    st16(c.rs, (u32x4){xres[4], xres[5], xres[6], xres[7]}, c.l16, base + 1024);
    f32x2 s2 = (y[0] + y[1]) + (y[2] + y[3]), q2 = y[0] * y[0];
    s2 += (y[4] + y[5]) + (y[6] + y[7]);
#pragma unroll
    for (int m = 1; m < 8; ++m) q2 += y[m] * y[m];
    const float sm = swap32_sum(s2[0] + s2[1]), sq = swap32_sum(q2[0] + q2[1]);
    if (lane < 32) st8(c.rs, (u32x2){__float_as_uint(sm), __float_as_uint(sq)}, lane * 128, st_off + (64 * s + 32 * tb) * 128 + (2 * j + nb) * 8);
}
DI void put16(unsigned a, const f32x16& v) {
#pragma unroll
    for (int m = 0; m < 4; ++m) *(LAS f32x4*)(uintptr_t)(a + m * 1024) = (f32x4){v[4 * m], v[4 * m + 1], v[4 * m + 2], v[4 * m + 3]};
}
DI void add16x3(unsigned a, f32x16& v) {
    f32x4 t[12];
#pragma unroll
    for (int m = 0; m < 12; ++m) t[m] = ldsf4(a + m * 1024);
#pragma unroll
    for (int m = 0; m < 4; ++m) { const f32x4 u = (t[m] + t[4 + m]) + t[8 + m]; v[4 * m] += u[0]; v[4 * m + 1] += u[1]; v[4 * m + 2] += u[2]; v[4 * m + 3] += u[3]; }
}

DI void parts_out(unsigned base, int nb, int q, int tbA, const f32x16& acc0, const f32x16& acc1) {
    const int sl = q > 0 ? q - 1 : 0;
    put16(base + ((nb + 2 * (1 - tbA)) * 3 + sl) * 4096, acc1);
    if (q >= 2) put16(base + (nb * 3 + sl) * 4096, acc0);
}
DI void phase_wo(const Ctx& c0, const Params& p, int l, int lp, int s, int j, unsigned (&xres)[8], u32x4 (&wo)[8], u32x4 (&wf)[32]) {
    OPAQUE_S(lp); OPAQUE_S(l); const Ctx c = fresh(c0); const int w = c.wid; OPAQUE_S(s); OPAQUE_S(j);
    const int nb = w & 1, q = w >> 1, tbA = q == 1 ? 1 : 0;
    if (w < 4) {
        const unsigned o = OFF_XT + ((2 * s + (w >> 1)) * 32 + 4 * j + 2 * nb) * 1024;
        const u32x4 a = ld16(c.rs, c.l16, o), b = ld16(c.rs, c.l16, o + 1024);
#pragma unroll
        for (int m = 0; m < 4; ++m) { xres[m] = a[m]; xres[4 + m] = b[m]; }
    }
    TSDECL; TS(0);
    await<1>(c, PH_ATT + lp, blk_attn(c.lane & 31, s >> 2));
    STAMP(4 * (3 + 5 * l) + 1); TS(1);
    const unsigned a_off = OFF_O + l * ACT_B + 2 * s * 32 * 1024;
    const int rot = rot32(s, j);
#define ISSUE(n) do { const unsigned o_ = 16 * ((n) >> 1) + 4 * (w & 3) + 2 * ((n) & 1); \
        dma<2>(c.rs, c.l16, fr32(a_off + (w >> 2) * 32768, o_, rot), c.lds0 + (w >> 2) * 32768 + o_ * 1024); } while (0)
    ISSUE(0); ORDER(); ISSUE(1); ISSUE(2); ISSUE(3); ORDER();
#undef ISSUE
    const unsigned nxt = w1_off(w, j);
    f32x16 acc0 = {}, acc1 = {};
    const unsigned ab0 = c.lds0 + c.l16 + q * 4096 + tbA * 32768, ab1 = c.lds0 + c.l16 + q * 4096 + (1 - tbA) * 32768;
#define COMPUTE(n) do { const unsigned o_ = (16 * ((n) >> 1) + 2 * ((n) & 1)) * 1024; u32x4 A0[2], A1[2]; \
        _Pragma("unroll") for (int i = 0; i < 2; ++i) { A0[i] = lds16(ab0 + o_ + i * 1024); A1[i] = lds16(ab1 + o_ + i * 1024); } SBAR(); \
        _Pragma("unroll") for (int i = 0; i < 2; ++i) { \
            acc0 = MFMA(asbf(wo[2 * (n) + i]), asbf(A0[i]), acc0); acc1 = MFMA(asbf(wo[2 * (n) + i]), asbf(A1[i]), acc1); SBAR(); } } while (0)
    WAITV_BAR(6); TS(2); COMPUTE(0);
    WAITV_BAR(0); COMPUTE(1); COMPUTE(2); COMPUTE(3);
#undef COMPUTE
    STAMP(4 * (3 + 5 * l) + 2); TS(3);
    parts_out(c.lds0 + 65536 + c.l16, nb, q, tbA, acc0, acc1);
    BAR();
    TS(4);
    if (w < 4) {
        const unsigned xa = c.lds0 + 65536 + w * 3 * 4096 + c.l16;
        add16x3(xa, acc0);
        res_finish(c, p, w, s, j, false, acc0, OFF_X1 + l * ACT_B, OFF_ST + (2 * l) * ST_B, xres);
    }
    TS(5);
    publish<0>(c, PH_WO + lp);
    TS(6);
#pragma unroll
    for (int i = W1_PRE + W1_MID; i < 32; ++i) wf[i] = ld16(c.rs, c.l16, fr32(nxt, i, rot));
    TS(7); if (l == 0) TSFLUSH(2);
}

#define W2MID 12
constexpr int W2_MID = W2MID;
DI unsigned w2_off(int w, int j) { return OFF_W2 + ((2 * j + (w & 1)) * 112 + 4 * (w >> 1)) * 1024; }
DI void phase_f1(const Ctx& c0, int l, int lp, int s, int j, u32x4 (&wf)[32]) {
    OPAQUE_S(lp); OPAQUE_S(l); const Ctx c = fresh(c0); const int w = c.wid; OPAQUE_S(s); OPAQUE_S(j);
    const int nb = 7 * j + w;
    f32x16 cs; load_cs(c, w < 7 ? OFF_CS1 + nb * 128 : OOB, cs);
    TSDECL; TS(0);
    await<1>(c, PH_WO + lp, blk_chain(s, c.lane & 7));
    TS(1);
    STAMP(4 * (4 + 5 * l) + 1);
    if (l == 0) XST(79);
    const int rot = rot32(s, j), r7 = rot7(s, j);
    issue_a512(c, w, OFF_X1 + l * ACT_B + 2 * s * 32 * 1024, OFF_ST + (2 * l) * ST_B + s * 8192, rot);
    constexpr int LATE1 = TRAIL ? 0 : 32 - W1_PRE - W1_MID;
    const unsigned ab = c.lds0 + c.l16, nxt = w2_off(w, j);
#define W2FRAG(i) ld16(c.rs, c.l16, nxt + (16 * chunk7((i) >> 2, r7) + ((i) & 3)) * 1024)
#define F1_WAIT(n) do { WAITV_BAR(((n) == 0 ? 6 : 0) + LATE1); if (l == 0) XST(80 + (n)); if ((n) == 0) TS(2); if ((n) == 1) TS(3); } while (0)
#define F1_OUT(tb) do { float z[16]; \
        _Pragma("unroll") for (int i = 0; i < 16; ++i) z[i] = (acc[i] - mu * cs[i]) * rs_; \
        const unsigned base = w < 7 ? OFF_H + l * H_B + ((2 * s + (tb)) * 112 + 2 * nb) * 1024 : OOB; \
        st16(c.rs, (u32x4){relupk(z[0], z[1]), relupk(z[2], z[3]), relupk(z[4], z[5]), relupk(z[6], z[7])}, c.l16, base); \
        st16(c.rs, (u32x4){relupk(z[8], z[9]), relupk(z[10], z[11]), relupk(z[12], z[13]), relupk(z[14], z[15])}, c.l16, base + 1024); } while (0)
    {
        f32x16 acc = {}; float mu, rs_; RowStat st;
#define F1_AFTER(ks) do { if ((ks) == 8) stat_read(c, 0, st); if ((ks) == 13) stat_finish(st, mu, rs_); } while (0)
        KLOOP1(ab, wf, acc, F1_WAIT, F1_AFTER);
#undef F1_AFTER
        F1_OUT(0);
    }
    {
        f32x16 acc = {}; float mu, rs_; RowStat st;
#define F1_AFTER(ks) do { if ((ks) < W2_MID) wf[ks] = W2FRAG(ks); if ((ks) == 22) stat_read(c, 1, st); if ((ks) == 27) stat_finish(st, mu, rs_); } while (0)
        KLOOP1(ab + 32768, wf, acc, NOTHING, F1_AFTER);
#undef F1_AFTER
        TS(4);
        STAMP(4 * (4 + 5 * l) + 2);
        F1_OUT(1);
    }
#undef F1_WAIT
#undef F1_OUT
    TS(5);
    publish<0>(c, PH_F1 + lp);
    TS(6);
#pragma unroll
    for (int i = W2_MID; i < 28; ++i) wf[i] = W2FRAG(i);
#undef W2FRAG
    TS(7); if (l == 0) TSFLUSH(3);
}

DI void phase_f2(const Ctx& c0, const Params& p, int l, int lp, int s, int j, unsigned (&xres)[8], u32x4 (&wf)[32]) {
    OPAQUE_S(lp); OPAQUE_S(l); const Ctx c = fresh(c0); const int w = c.wid; OPAQUE_S(s); OPAQUE_S(j);
    const int nb = w & 1, q = w >> 1, tbA = q == 1 ? 1 : 0;
    const bool last = l != 0;
    TSDECL; TS(0);
    await<1>(c, PH_F1 + lp, blk_chain(s, c.lane & 7));
    STAMP(4 * (5 + 5 * l) + 1); TS(1);
    const unsigned a_off = OFF_H + l * H_B + 2 * s * 112 * 1024;
    const int r7 = rot7(s, j), rot = rot32(s, j);
#define ISSUE(ch) dma<4>(c.rs, c.l16, a_off + ((w >> 2) * 112 + 16 * chunk7(ch, r7) + 4 * (w & 3)) * 1024, c.lds0 + ((ch) & 3) * 32768 + (w >> 2) * 16384 + 4 * (w & 3) * 1024)
    f32x16 acc0 = {}, acc1 = {};
    const unsigned ab0 = c.lds0 + c.l16 + q * 4096 + tbA * 16384, ab1 = c.lds0 + c.l16 + q * 4096 + (1 - tbA) * 16384;
#define COMPUTE(ch) do { const unsigned o_ = ((ch) & 3) * 32768; u32x4 A0[4], A1[4]; \
        _Pragma("unroll") for (int i = 0; i < 2; ++i) { A0[i] = lds16(ab0 + o_ + i * 1024); A1[i] = lds16(ab1 + o_ + i * 1024); } SBAR(); \
        _Pragma("unroll") for (int i = 0; i < 4; ++i) { \
            if (i < 2) { A0[i + 2] = lds16(ab0 + o_ + (i + 2) * 1024); A1[i + 2] = lds16(ab1 + o_ + (i + 2) * 1024); } SBAR(); \
            acc0 = MFMA(asbf(wf[4 * (ch) + i]), asbf(A0[i]), acc0); acc1 = MFMA(asbf(wf[4 * (ch) + i]), asbf(A1[i]), acc1); SBAR(); } } while (0)
    const unsigned nxt = wq_off(w, j);
    constexpr int P = QKV_P;
    ISSUE(0); ORDER(); ISSUE(1); ORDER(); ISSUE(2); ISSUE(3); ORDER();
    constexpr int R = TRAIL ? 0 : 28 - W2_MID;
    WAITV_BAR(12 + R); TS(2); COMPUTE(0);
    WAITV_BAR(8 + R); ISSUE(4); COMPUTE(1);
    WAITV_BAR(4 + R + 4); ISSUE(5); COMPUTE(2);
    WAITV_BAR(R + 8); ISSUE(6); COMPUTE(3);
    WAITV_BAR(8); COMPUTE(4);
    WAITV_BAR(4); COMPUTE(5);
    WAITV_BAR(0); TS(3); COMPUTE(6);
#undef ISSUE
#undef COMPUTE
    STAMP(4 * (5 + 5 * l) + 2);
    parts_out(c.lds0 + c.l16, nb, q, tbA, acc0, acc1);
    BAR();
    TS(4);
    if (w < 4) {
        const unsigned xa = c.lds0 + w * 3 * 4096 + c.l16;
        add16x3(xa, acc0);
        res_finish(c, p, w, s, j, last, acc0, OFF_XT, OFF_ST + ST_B, xres);
    }
    if (last) { __builtin_amdgcn_s_waitcnt(waitcnt_imm(0, 15)); return; }
    asm volatile("" ::: "memory");
#pragma unroll
    for (int i = 0; i < P; ++i) wf[i] = ld16(c.rs, c.l16, fr32(nxt, i, rot));
    TS(5);
    publish<P>(c, PH_F2);
    TS(6);
#pragma unroll
    for (int i = P; i < QKV_PRE; ++i) wf[i] = ld16(c.rs, c.l16, fr32(nxt, i, rot));
    TS(7); TSFLUSH(4);
}

__global__ void __launch_bounds__(NT, 2) fused_layers_kernel(Params p) {
    extern __shared__ __attribute__((aligned(16))) char lds[];
    Ctx c;
    c.ws = (GAS char*)p.ws; c.rs = mkrsrc(p.ws, WS_BYTES);
    c.lds0 = (unsigned)(uintptr_t)(LAS char*)lds;
    c.lane = threadIdx.x & 63; c.wid = __builtin_amdgcn_readfirstlane(threadIdx.x >> 6); c.l16 = c.lane * 16; c.b = blockIdx.x;
    c.tag = 0;
    const int x = c.b & 7, r = c.b >> 3, s = 4 * x + (r >> 3), j = r & 7;
    {
        u64 pc; asm volatile("s_getpc_b64 %0" : "=s"(pc));
        if (c.wid == 7) warm4k(mkrsrc((const void*)(pc & ~(u64)63), 37 * 1024), r < 10 ? r * 4096 + c.lane * 64 : OOB, 0, c.lds0 + LDS_DUMMY);
    }
    unsigned xres[8];
    u32x4 wf[32], wo[8];
    ESTAMP(0);
    phase_first(c, p, s, j);
    ESTAMP(7);
#pragma unroll 1
    for (int l_ = 0;; ++l_) {
        int l = __builtin_amdgcn_readfirstlane(l_); OPAQUE_S(l);
        int lp = PH_PER_LAYER * l; OPAQUE_S(lp);
        phase_attn(c, l, lp, s, j, wf, wo);
        ESTAMP(4 * (2 + 5 * l) + 3);
        phase_wo(c, p, l, lp, s, j, xres, wo, wf);
        ESTAMP(4 * (3 + 5 * l) + 3);
        phase_f1(c, l, lp, s, j, wf);
        ESTAMP(4 * (4 + 5 * l) + 3);
        phase_f2(c, p, l, lp, s, j, xres, wf);
        ESTAMP(4 * (5 + 5 * l) + 3);
        if (l != 0) break;
        phase_qkv1(c, s, j, wf);
        ESTAMP(4 * 6 + 3);
    }
    const Ctx ce = fresh(c);
    if (ce.b == 0 && ce.wid == 0 && ce.lane == 0) { const u64 t = ce.tag + 15; st8(ce.rs, (u32x2){(unsigned)t, (unsigned)(t >> 32)}, 0, OFF_EPOCH); }
}

}

extern "C" void kernel_launch(void* const* d_in, const int* in_sizes, int n_in, void* d_out, int out_size, void* d_ws, size_t ws_size, hipStream_t stream) {
    (void)in_sizes; (void)n_in; (void)out_size; (void)ws_size;
    Params p;
    p.x = d_in[0]; p.wqkv = d_in[1]; p.wo = d_in[2]; p.w1 = d_in[4]; p.w2 = d_in[5]; p.out = d_out; p.ws = d_ws;
    (void)hipFuncSetAttribute((const void*)fused_layers_kernel, hipFuncAttributeMaxDynamicSharedMemorySize, LDS_TOTAL);
    hipLaunchKernelGGL(fused_layers_kernel, dim3(256), dim3(NT), LDS_TOTAL, stream, p);
}
```

```cpp
#include <hip/hip_runtime.h>
#include <hip/hip_bf16.h>
#include <stdint.h>

namespace {

#define GAS __attribute__((address_space(1)))
#define LAS __attribute__((address_space(3)))
#define DI __device__ __forceinline__
typedef __bf16 bf16x8 __attribute__((ext_vector_type(8)));
typedef __bf16 bf16x2 __attribute__((ext_vector_type(2)));
typedef short s16x4 __attribute__((ext_vector_type(4)));
typedef float f32x2 __attribute__((ext_vector_type(2)));
typedef float f32x4 __attribute__((ext_vector_type(4)));
typedef float f32x16 __attribute__((ext_vector_type(16)));
typedef unsigned u32x2 __attribute__((ext_vector_type(2)));
typedef unsigned u32x4 __attribute__((ext_vector_type(4)));
typedef unsigned long long u64;
typedef __amdgpu_buffer_rsrc_t rsrc_t;

#define P0TOUCH 0
#define TRAIL 1
#define WARM 0
constexpr int NT = 512;
constexpr unsigned OFF_EPOCH = 0;
constexpr unsigned OFF_FLAGS = 0x1000;
constexpr unsigned OFF_PROF  = 0x2D00000;
constexpr unsigned OFF_CSQ   = 0x70000;
constexpr unsigned OFF_CS1   = 0x72000;
constexpr unsigned OFF_ST    = 0x2C00000;
constexpr unsigned OFF_WQ    = 0x100000;
constexpr unsigned OFF_WO    = 0x280000;
constexpr unsigned OFF_W1    = 0x300000;
constexpr unsigned OFF_W2    = 0x4C0000;
constexpr unsigned OFF_XT    = 0x700000;
constexpr unsigned OFF_Q     = 0xA00000;
constexpr unsigned OFF_K     = 0xE00000;
constexpr unsigned OFF_V     = 0x1200000;
constexpr unsigned OFF_O     = 0x1600000;
constexpr unsigned OFF_X1    = 0x1A00000;
constexpr unsigned OFF_H     = 0x1E00000;
constexpr unsigned WS_BYTES  = 0x2E80000;
constexpr unsigned ACT_B = 0x200000, H_B = 0x700000, ST_B = 0x40000;
constexpr int PH_PREP = 0, PH_QKV = 1, PH_ATT = 2, PH_WO = 3, PH_F1 = 4, PH_F2 = 5, PH_PER_LAYER = 5;
constexpr int ATT_UNR = 7;
constexpr int LDS_K = 0, LDS_V = 65536, LDS_ONES = 131072, ONES_BYTES = (ATT_UNR + 2) * 1024 + 256;
constexpr int LDS_XCH = LDS_ONES + ONES_BYTES;
constexpr int LDS_FLAG = LDS_XCH + 8 * 2304;
constexpr int LDS_DUMMY = LDS_FLAG + 64;
constexpr int LDS_TOTAL = LDS_DUMMY + 4096;
static_assert(LDS_TOTAL <= 163840, "LDS");

struct Params { const void* x; const void* wqkv; const void* wo; const void* w1; const void* w2; void* out; void* ws; };

#define MFMA(a, b, c) __builtin_amdgcn_mfma_f32_32x32x16_bf16((a), (b), (c), 0, 0, 0)
#define SBAR() __builtin_amdgcn_sched_barrier(0)
#define BAR() asm volatile("s_waitcnt lgkmcnt(0)\n\ts_barrier" ::: "memory")
constexpr int waitcnt_imm(int vm, int lgkm) { return (vm & 15) | ((vm >> 4) << 14) | (7 << 4) | (lgkm << 8); }
#define WAITV_BAR(n) do { asm volatile("" ::: "memory"); __builtin_amdgcn_s_waitcnt(waitcnt_imm(n, 0)); asm volatile("s_barrier" ::: "memory"); } while (0)
#define ORDER() asm volatile("s_barrier" ::: "memory")
#define DRAIN() asm volatile("s_waitcnt vmcnt(0)" ::: "memory")

DI unsigned cvtpk(float lo, float hi) { f32x2 v = {lo, hi}; return __builtin_bit_cast(unsigned, __builtin_convertvector(v, bf16x2)); }
DI unsigned relupk(float lo, float hi) { typedef short s16x2 __attribute__((ext_vector_type(2)));
    const s16x2 v = __builtin_bit_cast(s16x2, cvtpk(lo, hi)); return __builtin_bit_cast(unsigned, __builtin_elementwise_max(v, (s16x2){0, 0})); }
DI bf16x8 asbf(u32x4 v) { return __builtin_bit_cast(bf16x8, v); }
DI float bflo(unsigned u) { return __uint_as_float(u << 16); }
DI float bfhi(unsigned u) { return __uint_as_float(u & 0xffff0000u); }
DI rsrc_t mkrsrc(const void* p, unsigned bytes) { return __builtin_amdgcn_make_buffer_rsrc((void*)p, 0, bytes, 0x00020000); }
DI u32x4 ld16(rsrc_t rs, unsigned voff, unsigned soff) { return __builtin_amdgcn_raw_buffer_load_b128(rs, voff, soff, 16); }
DI unsigned ld4(rsrc_t rs, unsigned voff, unsigned soff) { return __builtin_amdgcn_raw_buffer_load_b32(rs, voff, soff, 16); }
DI void st16(rsrc_t rs, u32x4 v, unsigned voff, unsigned soff) { __builtin_amdgcn_raw_buffer_store_b128(v, rs, voff + soff, 0, 16); }
DI void st8(rsrc_t rs, u32x2 v, unsigned voff, unsigned soff) { __builtin_amdgcn_raw_buffer_store_b64(v, rs, voff, soff, 16); }
DI void st4(rsrc_t rs, unsigned v, unsigned voff, unsigned soff) { __builtin_amdgcn_raw_buffer_store_b32(v, rs, voff, soff, 16); }
#define DMA_HEAD "s_nop 2\n\ts_mov_b32 m0, %0\n\ts_nop 0\n\t"
#define DMA_ONE(off, pol) "buffer_load_dwordx4 %1, %2, %3 offen offset:" #off pol " lds\n\t"
template <int N> DI void dma(rsrc_t rs, unsigned voff, unsigned soff, unsigned ldsdst) {
    if constexpr (N == 1) asm volatile(DMA_HEAD DMA_ONE(0, " sc1") :: "s"(ldsdst), "v"(voff), "s"(rs), "s"(soff) : "memory");
    if constexpr (N == 2) asm volatile(DMA_HEAD DMA_ONE(0, " sc1") DMA_ONE(1024, " sc1") :: "s"(ldsdst), "v"(voff), "s"(rs), "s"(soff) : "memory");
    if constexpr (N == 3) asm volatile(DMA_HEAD DMA_ONE(0, " sc1") DMA_ONE(1024, " sc1") DMA_ONE(2048, " sc1") :: "s"(ldsdst), "v"(voff), "s"(rs), "s"(soff) : "memory");
    if constexpr (N == 4) asm volatile(DMA_HEAD DMA_ONE(0, " sc1") DMA_ONE(1024, " sc1") DMA_ONE(2048, " sc1") DMA_ONE(3072, " sc1") :: "s"(ldsdst), "v"(voff), "s"(rs), "s"(soff) : "memory");
}
DI void dma_in(rsrc_t rs, unsigned voff, unsigned soff, unsigned ldsdst) {
    asm volatile(DMA_HEAD DMA_ONE(0, "") :: "s"(ldsdst), "v"(voff), "s"(rs), "s"(soff) : "memory");
}
DI void warm4k(rsrc_t rs, unsigned l64, unsigned soff, unsigned ldsdst) {
    asm volatile("s_nop 2\n\ts_mov_b32 m0, %0\n\ts_nop 0\n\tbuffer_load_dword %1, %2, %3 offen lds" :: "s"(ldsdst), "v"(l64), "s"(rs), "s"(soff) : "memory");
}
#define OPAQUE_S(x) asm volatile("" : "+s"(x))
#define PIN4(a, i) asm volatile("" : "+v"(a[i]), "+v"(a[(i) + 1]), "+v"(a[(i) + 2]), "+v"(a[(i) + 3]))
DI u32x4 lds16(unsigned a) { return *(const LAS u32x4*)(uintptr_t)a; }
DI f32x4 ldsf4(unsigned a) { return *(const LAS f32x4*)(uintptr_t)a; }
DI s16x4 ldstr(unsigned a) { return __builtin_amdgcn_ds_read_tr16_b64_v4i16((LAS s16x4*)(uintptr_t)a); }
DI bf16x8 cat(s16x4 a, s16x4 b) { return __builtin_bit_cast(bf16x8, __builtin_shufflevector(a, b, 0, 1, 2, 3, 4, 5, 6, 7)); }
template <int CTRL> DI float dpp(float x) { return __builtin_bit_cast(float, __builtin_amdgcn_mov_dpp(__builtin_bit_cast(int, x), CTRL, 0xf, 0xf, true)); }
DI float swap32_sum(float x) { auto r = __builtin_amdgcn_permlane32_swap(__float_as_uint(x), __float_as_uint(x), false, false); return __uint_as_float(r[0]) + __uint_as_float(r[1]); }
DI float swap32_max(float x) { auto r = __builtin_amdgcn_permlane32_swap(__float_as_uint(x), __float_as_uint(x), false, false); return fmaxf(__uint_as_float(r[0]), __uint_as_float(r[1])); }
DI float swap32_lo(float x) { auto r = __builtin_amdgcn_permlane32_swap(__float_as_uint(x), __float_as_uint(x), false, false); return __uint_as_float(r[0]); }
DI float swap16_max(float x) { auto r = __builtin_amdgcn_permlane16_swap(__float_as_uint(x), __float_as_uint(x), false, false); return fmaxf(__uint_as_float(r[0]), __uint_as_float(r[1])); }
DI float swap16_sum(float x) { auto r = __builtin_amdgcn_permlane16_swap(__float_as_uint(x), __float_as_uint(x), false, false); return __uint_as_float(r[0]) + __uint_as_float(r[1]); }
DI float max32(float x) {
    x = fmaxf(x, dpp<0xB1>(x)); x = fmaxf(x, dpp<0x4E>(x)); x = fmaxf(x, dpp<0x141>(x)); x = fmaxf(x, dpp<0x140>(x)); return swap16_max(x); }
DI float sum8(float x) { x += dpp<0xB1>(x); x += dpp<0x4E>(x); x += dpp<0x141>(x); return x; }
DI float sum64(float x) { x = sum8(x); x += dpp<0x140>(x); x = swap16_sum(x); return swap32_sum(x); }

#define STAMP(i) do { } while (0)
#define ESTAMP(i) do { } while (0)
#define WSTAMP(i) do { } while (0)
#define XST(i) do { } while (0)

#define TSDECL do { } while (0)
#define TS(i) do { } while (0)
#define TSFLUSH(base) do { } while (0)
struct Ctx {
    GAS char* ws; rsrc_t rs;
    u64 tag;
    unsigned lds0, l16;
    int lane, wid, b;
};
DI Ctx fresh(const Ctx& c0) {
    Ctx c = c0;
    int lane = __builtin_amdgcn_mbcnt_hi(~0u, __builtin_amdgcn_mbcnt_lo(~0u, 0u));
    asm volatile("" : "+v"(lane));
    c.lane = lane; c.l16 = lane * 16;
    asm volatile("" : "+s"(c.wid), "+s"(c.b), "+s"(c.lds0));
    return c;
}
DI int blk_chain(int s, int j) { return (s >> 2) + 8 * ((s & 3) * 8 + j); }
DI int blk_attn(int h, int g) { return (h & 7) + 8 * (4 * g + (h >> 3)); }
#define ROT 0
DI int rot32(int s, int j) { return ROT ? (4 * j + 8 * (s & 3)) & 31 : 0; }
DI int rot7(int s, int j) { const int t = j + 2 * (s & 3); return ROT ? (t >= 7 ? (t >= 14 ? t - 14 : t - 7) : t) : 0; }
DI int chunk7(int n, int r) { const int t = n + r; return t >= 7 ? t - 7 : t; }
DI unsigned fr32(unsigned base, int n, int rot) { return base + ((n + rot) & 31) * 1024; }

template <int N> DI void publish(const Ctx& c, int phase) {
    WAITV_BAR(N);
    if (c.wid == 0 && c.lane == 0) { u64 t = c.tag; asm volatile("" : "+s"(t)); t += (u64)phase; st8(c.rs, (u32x2){(unsigned)t, (unsigned)(t >> 32)}, 0, OFF_FLAGS + (phase * 256 + c.b) * 128); }
}
template <int PER_LANE> DI void await(const Ctx& c, int phase, int pb) {
    if (c.wid == 0) {
        u64 want = c.tag; asm volatile("" : "+s"(want)); want += (u64)phase;
        for (unsigned spins = 0; spins < (1u << 18); ++spins) {
            asm volatile("" ::: "memory");
            bool ok = true;
#pragma unroll
            for (int i = 0; i < PER_LANE; ++i) { const u32x2 f = __builtin_amdgcn_raw_buffer_load_b64(c.rs, pb * 128, OFF_FLAGS + (phase * 256 + 64 * i) * 128, 16);
                ok &= f[0] == (unsigned)want && f[1] == (unsigned)(want >> 32); }
            if (__all(ok)) break;
        }
    }
    BAR();
}

constexpr unsigned OOB = 0xF0000000u;

DI void qkv_store(const Ctx& c, const f32x16& acc, const f32x16& cs, float mu, float rstd, int kind, int pi, int l, int s, int j, int tb) {
    if (kind == 0) rstd *= 0.25f * 1.4426950408889634f;
    float z[16];
#pragma unroll
    for (int i = 0; i < 16; ++i) z[i] = (acc[i] - mu * cs[i]) * rstd;
    const int tbg = 2 * s + tb, hA = 4 * j + 2 * pi;
    const unsigned base = (kind == 0 ? OFF_Q : kind == 1 ? OFF_K : OFF_V) + l * ACT_B + (hA * 64 + tbg) * 1024;
    const unsigned vo = kind == 2 ? (c.lane & 31) * 32 + (c.lane >> 5) * 16 : c.l16;
    st16(c.rs, (u32x4){cvtpk(z[0], z[1]), cvtpk(z[2], z[3]), cvtpk(z[4], z[5]), cvtpk(z[6], z[7])}, vo, base);
    st16(c.rs, (u32x4){cvtpk(z[8], z[9]), cvtpk(z[10], z[11]), cvtpk(z[12], z[13]), cvtpk(z[14], z[15])}, vo, base + 64 * 1024);
}

DI void phase_first(Ctx& c0, const Params& p, int s, int j) {
    Ctx c = fresh(c0); const int lane = c.lane, w = c.wid, b = c.b;
    TSDECL; TS(0);
    const rsrc_t rx = mkrsrc(p.x, 2048 * 512 * 2), rq = mkrsrc(p.wqkv, 512 * 1536 * 2);
    const unsigned vX = (lane >> 3) * 1024 + (((lane & 7) ^ ((((8 * w + (lane >> 3)) & 31) >> 1) & 7)) * 16);
    const unsigned vW = (lane >> 3) * 3072 + (((lane & 7) ^ (((lane >> 4) & 1) << 2)) * 16);
    const int rot8 = ROT ? (j + 2 * (s & 3)) & 7 : 0;
#define FISSUE(ch) do { const unsigned sb_ = c.lds0 + ((ch) & 3) * 32768; const int ca_ = ((ch) + rot8) & 7; \
        dma_in(rx, vX, (64 * s + 8 * w) * 1024 + ca_ * 128, sb_ + w * 1024); \
        _Pragma("unroll") for (int i = 0; i < 3; ++i) { const int pi_ = 3 * w + i; \
            dma_in(rq, vW, (64 * ca_ + 8 * (pi_ & 7)) * 3072 + ((pi_ >> 3) * 512 + 64 * j) * 2, sb_ + 8192 + pi_ * 1024); } } while (0)
    FISSUE(0); ORDER(); FISSUE(1); FISSUE(2); FISSUE(3);
    constexpr int NT0 = 0;
    {
        u64 e; asm volatile("s_load_dwordx2 %0, %1, 0x0\n\ts_waitcnt lgkmcnt(0)" : "=s"(e) : "s"(p.ws) : "memory");
        c.tag = c0.tag = e + 1;
    }
    XST(86); TS(1);
    const int nb = w < 4 ? w : 4 + (w & 1), r = lane & 31, h2 = lane >> 5;
    unsigned xa[4];
#pragma unroll
    for (int k = 0; k < 4; ++k) xa[k] = (r >> 3) * 1024 + (r & 7) * 128 + (((2 * k + h2) ^ ((r >> 1) & 7)) * 16) + (w < 4 ? 0 : ((w >> 1) & 1) * 4096);
    unsigned wa;
    { const int g = lane >> 4, q = (lane & 15) >> 2, pp = lane & 3;
      wa = 8192 + (nb >> 1) * 8192 + (8 * (g >> 1) + q) * 128 + ((4 * (nb & 1) + 2 * (g & 1) + (pp >> 1)) ^ ((q >> 1) << 2)) * 16 + (pp & 1) * 8; }
    const bf16x8 ones = asbf((u32x4){0x3f803f80u, 0x3f803f80u, 0x3f803f80u, 0x3f803f80u});
    f32x16 acc0 = {}, acc1 = {}, cs = {};
    float sm = 0.f, sq = 0.f;
    u32x2 xg[4] = {};
    bf16x8 wfr[2][4]; u32x4 x0[2][4], x1[2][4], xr[2];
    const unsigned xo1 = w < 4 ? 4096 : 0;
#define FREAD1(ch, k) do { const unsigned sb_ = c.lds0 + ((ch) & 3) * 32768; \
        wfr[(ch) & 1][k] = cat(ldstr(sb_ + wa + (k) * 2048), ldstr(sb_ + wa + (k) * 2048 + 512)); x0[(ch) & 1][k] = lds16(sb_ + xa[k]); x1[(ch) & 1][k] = lds16(sb_ + xa[k] + xo1); \
        if ((k) == 0) xr[(ch) & 1] = lds16(sb_ + w * 1024 + c.l16); \
        if ((k) == 3 && w < 4 && (((ch) + rot8) & 7) == j) {        \
            const unsigned t_ = 32 * (w >> 1) + r, a_ = sb_ + (t_ >> 3) * 1024 + (t_ & 7) * 128 + h2 * 8; \
            _Pragma("unroll") for (int m = 0; m < 4; ++m) xg[m] = *(const LAS u32x2*)(uintptr_t)(a_ + (((4 * (w & 1) + m) ^ ((r >> 1) & 7)) * 16)); } } while (0)
#define FISSUE1(ch, k) do { const unsigned sb_ = c.lds0 + ((ch) & 3) * 32768; const int ca_ = ((ch) + rot8) & 7; \
        if ((k) == 0) dma_in(rx, vX, (64 * s + 8 * w) * 1024 + ca_ * 128, sb_ + w * 1024); \
        else { const int pi_ = 3 * w + (k) - 1; dma_in(rq, vW, (64 * ca_ + 8 * (pi_ & 7)) * 3072 + ((pi_ >> 3) * 512 + 64 * j) * 2, sb_ + 8192 + pi_ * 1024); } } while (0)
#define FSTEP(cm, rd, ci) do { \
        _Pragma("unroll") for (int k = 0; k < 4; ++k) { \
            if ((cm) >= 0) { acc0 = MFMA(wfr[(cm) & 1][k], asbf(x0[(cm) & 1][k]), acc0); \
                if (w < 4) acc1 = MFMA(wfr[(cm) & 1][k], asbf(x1[(cm) & 1][k]), acc1); \
                cs = MFMA(wfr[(cm) & 1][k], ones, cs); \
                const float lo = bflo(xr[(cm) & 1][k]), hi = bfhi(xr[(cm) & 1][k]); sm += lo + hi; sq += lo * lo + hi * hi; \
                asm volatile("" : "+v"(acc0), "+v"(acc1), "+v"(cs)); } \
            SBAR(); \
            if (rd) FREAD1((cm) + 1, k); \
            SBAR(); \
            if ((ci) >= 0) FISSUE1((ci) & 7, k); \
            SBAR(); } } while (0)
    rsrc_t rw = rq; unsigned ld = 1536, col0 = 0, ks0 = 4 * w, dst = OOB, csdst = OOB, src0 = OOB;
    if (b < 48) { const int jj = b / 6, i = b % 6; col0 = (i >> 1) * 512 + 64 * jj + 32 * (i & 1); dst = OFF_WQ + (b * 32 + ks0) * 1024; csdst = OFF_CSQ + b * 128; src0 = 0; }
    else if (b < 64) { rw = mkrsrc(p.wo, 512 * 512 * 2); ld = 512; col0 = 32 * (b - 48); dst = OFF_WO + ((b - 48) * 32 + ks0) * 1024; src0 = 0; }
    else if (b < 120) { rw = mkrsrc(p.w1, 512 * 1792 * 2); ld = 1792; col0 = 32 * (b - 64); dst = OFF_W1 + ((b - 64) * 32 + ks0) * 1024; csdst = OFF_CS1 + (b - 64) * 128; src0 = 0; }
    else if (b < 176) { rw = mkrsrc(p.w2, 1792 * 512 * 2); ld = 512; const int F = 32 * (b - 120) + 4 * w; col0 = 32 * (F / 112); ks0 = F % 112; dst = OFF_W2 + F * 1024; src0 = 0; }
    const unsigned stg = c.lds0 + w * 4096;
    WAITV_BAR(12 + NT0); XST(89); TS(2); FSTEP(-1, true, -1);
    WAITV_BAR(8 + NT0);  XST(90); FSTEP(0, true, 4);
    WAITV_BAR(8 + NT0);  XST(91); FSTEP(1, true, 5);
    WAITV_BAR(8 + NT0);  XST(92); FSTEP(2, true, 6);
    WAITV_BAR(8);  XST(93); TS(3); FSTEP(3, true, 7);
    WAITV_BAR(8);  XST(94);
    {
        const unsigned voff = ((lane >> 2) * ld + col0) * 2 + (lane & 3) * 16;
#pragma unroll
        for (int i = 0; i < 4; ++i) dma_in(rw, voff, src0 + (ks0 + i) * 16 * ld * 2, stg + i * 1024);
    }
    FSTEP(4, true, -1);
    WAITV_BAR(8);  XST(95); FSTEP(5, true, -1);
    WAITV_BAR(4);  XST(96); TS(4); FSTEP(6, true, -1);
    FSTEP(7, false, -1);
#undef FREAD1
#undef FISSUE1
#undef FISSUE
#undef FSTEP
    STAMP(4 * 1 + 2); TS(5);
    sm = sum8(sm); sq = sum8(sq);
    if ((lane & 7) == 0) {
        const float mu = sm * (1.0f / 512.0f), var = fmaxf(sq * (1.0f / 512.0f) - mu * mu, 0.f);
        *(LAS f32x2*)(uintptr_t)(c.lds0 + LDS_XCH + (8 * w + (lane >> 3)) * 8) = (f32x2){mu, __builtin_amdgcn_rsqf(var + 1e-5f)};
    }
    BAR();
    if (w < 4) {
        const unsigned o_ = OFF_XT + ((2 * s + (w >> 1)) * 32 + 4 * j + 2 * (w & 1)) * 1024;
        st16(c.rs, (u32x4){xg[0][0], xg[0][1], xg[1][0], xg[1][1]}, c.l16, o_); st16(c.rs, (u32x4){xg[2][0], xg[2][1], xg[3][0], xg[3][1]}, c.l16, o_ + 1024);
        const f32x2 t0 = *(const LAS f32x2*)(uintptr_t)(c.lds0 + LDS_XCH + r * 8), t1 = *(const LAS f32x2*)(uintptr_t)(c.lds0 + LDS_XCH + (32 + r) * 8);
        qkv_store(c, acc0, cs, t0[0], t0[1], w >> 1, w & 1, 0, s, j, 0);
        qkv_store(c, acc1, cs, t1[0], t1[1], w >> 1, w & 1, 0, s, j, 1);
    } else {
        const int tb = (w >> 1) & 1;
        const f32x2 t0 = *(const LAS f32x2*)(uintptr_t)(c.lds0 + LDS_XCH + (32 * tb + r) * 8);
        qkv_store(c, acc0, cs, t0[0], t0[1], 2, w & 1, 0, s, j, tb);
    }
    TS(6);
    publish<0>(c, PH_QKV);
    TS(7); TSFLUSH(0);
    {
        const int g = lane >> 4, hh = g >> 1, nh = g & 1, q = (lane & 15) >> 2, pp = lane & 3;
        const unsigned ta = stg + (4 * hh + q) * 64 + (16 * nh + 4 * pp) * 2;
        f32x16 cw = {};
#pragma unroll
        for (int i = 0; i < 4; ++i) {
            const bf16x8 f = cat(ldstr(ta + i * 1024), ldstr(ta + i * 1024 + 512));
            st16(c.rs, __builtin_bit_cast(u32x4, f), c.l16, dst + i * 1024);
            cw = MFMA(f, ones, cw);
        }
        if ((lane & 31) == 0) {
#pragma unroll
            for (int m = 0; m < 4; ++m) *(LAS f32x4*)(uintptr_t)(c.lds0 + LDS_XCH + 1024 + (w * 2 + (lane >> 5)) * 64 + m * 16) = (f32x4){cw[4 * m], cw[4 * m + 1], cw[4 * m + 2], cw[4 * m + 3]};
        }
        BAR();
        if (w == 0 && lane < 32) { float a = 0.f;
#pragma unroll
            for (int ww = 0; ww < 8; ++ww) a += *(const LAS float*)(uintptr_t)(c.lds0 + LDS_XCH + 1024 + ww * 128 + lane * 4);
            st4(c.rs, __float_as_uint(a), lane * 4, csdst); }
        publish<0>(c, PH_PREP);
    }
}

DI void issue_a512(const Ctx& c, int w, unsigned a_off  , unsigned st_off, int rot) {
    const unsigned tbo = (w >> 2) * 32768, n0 = 2 * (w & 3);
    dma<1>(c.rs, c.l16, st_off + w * 1024, c.lds0 + LDS_XCH + w * 1024);
    dma<2>(c.rs, c.l16, fr32(a_off + tbo, n0, rot), c.lds0 + tbo + n0 * 1024);
    ORDER();
#pragma unroll
    for (int ch = 1; ch < 4; ++ch) dma<2>(c.rs, c.l16, fr32(a_off + tbo, n0 + 8 * ch, rot), c.lds0 + tbo + (n0 + 8 * ch) * 1024);
    ORDER();
}
struct RowStat { f32x4 u0, u1, u2, u3; };
DI void stat_read(const Ctx& c, int tb, RowStat& t) {
    const unsigned r = c.lane & 31, a = c.lds0 + LDS_XCH + (32 * tb + r) * 128, k = ((4 * (c.lane >> 5)) ^ ((r >> 1) & 7)) * 16;
    t.u0 = ldsf4(a + k); t.u1 = ldsf4(a + (k ^ 16)); t.u2 = ldsf4(a + (k ^ 32)); t.u3 = ldsf4(a + (k ^ 48));
}
DI void stat_finish(const RowStat& t, float& mu, float& rstd) {
    const float sm = swap32_sum(((t.u0[0] + t.u0[2]) + (t.u1[0] + t.u1[2])) + ((t.u2[0] + t.u2[2]) + (t.u3[0] + t.u3[2])));
    const float sq = swap32_sum(((t.u0[1] + t.u0[3]) + (t.u1[1] + t.u1[3])) + ((t.u2[1] + t.u2[3]) + (t.u3[1] + t.u3[3])));
    mu = sm * (1.0f / 512.0f);
    rstd = __builtin_amdgcn_rsqf(fmaxf(sq * (1.0f / 512.0f) - mu * mu, 0.f) + 1e-5f);
}
#define KLOOP2(ab, wf, acc0, acc1, WAIT, AFTER) do { \
        u32x4 A0[3], A1[3]; \
        WAIT(0); \
        A0[0] = lds16((ab)); A1[0] = lds16((ab) + 32768); A0[1] = lds16((ab) + 1024); A1[1] = lds16((ab) + 33792); SBAR(); \
        _Pragma("unroll") for (int ks = 0; ks < 32; ++ks) { \
            if (ks == 6) { WAIT(1); } \
            if (ks + 2 < 32) { A0[(ks + 2) % 3] = lds16((ab) + (ks + 2) * 1024); A1[(ks + 2) % 3] = lds16((ab) + 32768 + (ks + 2) * 1024); } \
            SBAR(); \
            acc0 = MFMA(asbf(wf[ks]), asbf(A0[ks % 3]), acc0); acc1 = MFMA(asbf(wf[ks]), asbf(A1[ks % 3]), acc1); SBAR(); \
            AFTER(ks); SBAR(); \
        } } while (0)
#define KLOOP1(ab, wf, acc0, WAIT, AFTER) do { \
        u32x4 A0[4]; \
        WAIT(0); \
        A0[0] = lds16((ab)); A0[1] = lds16((ab) + 1024); A0[2] = lds16((ab) + 2048); SBAR(); \
        _Pragma("unroll") for (int ks = 0; ks < 32; ++ks) { \
            if (ks == 5) { WAIT(1); } \
            if (ks + 3 < 32) A0[(ks + 3) % 4] = lds16((ab) + (ks + 3) * 1024); \
            SBAR(); \
            acc0 = MFMA(asbf(wf[ks]), asbf(A0[ks % 4]), acc0); SBAR(); \
            AFTER(ks); SBAR(); \
        } } while (0)
#define NOTHING(x) do { } while (0)
DI void load_cs(const Ctx& c, unsigned off, f32x16& cs) {
#pragma unroll
    for (int m = 0; m < 4; ++m) { const f32x4 t = __builtin_bit_cast(f32x4, ld16(c.rs, (c.lane >> 5) * 64 + m * 16, off));
        cs[4 * m] = t[0]; cs[4 * m + 1] = t[1]; cs[4 * m + 2] = t[2]; cs[4 * m + 3] = t[3]; }
}

#define QKVP 10
#define QKVPRE 16
constexpr int QKV_P = QKVP, QKV_PRE = QKVPRE;
DI unsigned wq_off(int w, int j) { return OFF_WQ + (6 * j + (w < 4 ? w : 4 + (w & 1))) * 32 * 1024; }
DI void phase_qkv1(const Ctx& c0, int s, int j, u32x4 (&wf)[32]) {
    const Ctx c = fresh(c0); const int w = c.wid; OPAQUE_S(s); OPAQUE_S(j);
    const int nb = w < 4 ? w : 4 + (w & 1);
    const unsigned wo_ = wq_off(w, j); const int rot = rot32(s, j);
    f32x16 cs; load_cs(c, OFF_CSQ + (6 * j + nb) * 128, cs);
    TSDECL; TS(0);
    await<1>(c, PH_F2, blk_chain(s, c.lane & 7));
    STAMP(4 * 6 + 1); TS(1);
    issue_a512(c, w, OFF_XT + 2 * s * 32 * 1024, OFF_ST + ST_B + s * 8192, rot);
    const unsigned ab = c.lds0 + c.l16;
#define QKV_WAIT(n) do { WAITV_BAR((n) == 0 ? 6 : 5); if ((n) == 0) TS(2); else TS(3); } while (0)
#define QKV_LATE(ks) do { if (QKV_PRE + (ks) < 32) wf[(QKV_PRE + (ks)) & 31] = ld16(c.rs, c.l16, fr32(wo_, QKV_PRE + (ks), rot)); } while (0)
    {
        const int tb = w < 4 ? 0 : (w >> 1) & 1;
        f32x16 acc = {}; float mu, rs_; RowStat st;
#define QKV_AFTER(ks) do { QKV_LATE(ks); if ((ks) == 8) stat_read(c, tb, st); if ((ks) == 13) stat_finish(st, mu, rs_); } while (0)
        KLOOP1(ab + tb * 32768, wf, acc, QKV_WAIT, QKV_AFTER);
#undef QKV_AFTER
        qkv_store(c, acc, cs, mu, rs_, w < 4 ? w >> 1 : 2, w & 1, 1, s, j, tb);
    }
    if (w < 4) {
        f32x16 acc = {}; float mu, rs_; RowStat st;
#define QKV_AFTER(ks) do { if ((ks) == 2) stat_read(c, 1, st); if ((ks) == 7) stat_finish(st, mu, rs_); } while (0)
        KLOOP1(ab + 32768, wf, acc, NOTHING, QKV_AFTER);
#undef QKV_AFTER
        STAMP(4 * 6 + 2); TS(4);
        qkv_store(c, acc, cs, mu, rs_, w >> 1, w & 1, 1, s, j, 1);
    }
#undef QKV_WAIT
#undef QKV_LATE
    TS(5);
    publish<0>(c, PH_QKV + PH_PER_LAYER);
    TS(7); TSFLUSH(5);
}

#define ATT_PRIO 1
#define ATT_PRIO2 0
#define ATT_PRIO_END 16
#define W1PRE 22
#define W1MID 10
constexpr int W1_PRE = W1PRE, W1_MID = W1MID;
DI unsigned w1_off(int w, int j) { return w < 7 ? OFF_W1 + (7 * j + w) * 32 * 1024 : OOB; }
DI void phase_attn(const Ctx& c0, int l, int lp, int s, int j, u32x4 (&wf)[32], u32x4 (&wo)[8]) {
    OPAQUE_S(lp); OPAQUE_S(l); const Ctx c = fresh(c0); const int wid = c.wid, bb = c.b; OPAQUE_S(j); OPAQUE_S(s);
    const int lane = c.lane, h = (bb & 7) + 8 * ((bb >> 3) & 3), g = bb >> 5;
    const int qp = wid & 3, kh = wid >> 2;
    TSDECL; TS(0);
    __builtin_amdgcn_s_waitcnt(waitcnt_imm(0, 15));
    for (int i = wid * 64 + lane; i < ONES_BYTES / 4; i += NT) *(LAS unsigned*)(uintptr_t)(c.lds0 + LDS_ONES + i * 4) = (i & 7) == 0 ? 0x00003f80u : 0u;
    await<1>(c, PH_QKV + lp, blk_chain(lane & 31, h >> 2));
    STAMP(4 * (2 + 5 * l) + 1); TS(1);
    const unsigned kb = OFF_K + l * ACT_B + h * 65536, vb = OFF_V + l * ACT_B + h * 65536;
    u32x4 qrA, qrB;
    {
        const unsigned qo = OFF_Q + l * ACT_B + (h * 64 + 8 * g + 2 * qp) * 1024;
        asm volatile("s_nop 4\n\tbuffer_load_dwordx4 %0, %2, %3, %4 offen sc1\n\tbuffer_load_dwordx4 %1, %2, %3, %4 offen offset:1024 sc1"
                     : "=&v"(qrA), "=&v"(qrB) : "v"(c.l16), "s"(c.rs), "s"(qo) : "memory");
    }
    {
        const unsigned sk = c.lds0 + LDS_K + kh * 32768, sv = c.lds0 + LDS_V + kh * 32768;
        const unsigned tA = 8 * g + 4 * kh + qp, i0 = 8 * g + 8 + 28 * kh;
        dma<1>(c.rs, c.l16, kb + tA * 1024, sk + qp * 1024);
        dma<1>(c.rs, c.l16, vb + tA * 1024, sv + qp * 1024);
        ORDER();
        const unsigned tB = (i0 + 2 * qp) & 63;
        dma<2>(c.rs, c.l16, kb + tB * 1024, sk + (4 + 2 * qp) * 1024);
        dma<2>(c.rs, c.l16, vb + tB * 1024, sv + (4 + 2 * qp) * 1024);
    }
    TS(6);
    asm volatile("s_waitcnt vmcnt(6)" : "+v"(qrA), "+v"(qrB) :: "memory");
    TS(5);
    float mref = 0.f, mB = 0.f;
    const bf16x8 qA = asbf(qrA), qB = asbf(qrB);
    const int grp = lane >> 4, h2 = grp >> 1, isone = grp & 1, qq = (lane & 15) >> 2, pp = lane & 3;
    const int inrow = qq * 32 + (pp & 1) * 16 + (pp >> 1) * 8;
    const int vstep = isone ? 0 : ATT_UNR * 1024, vstep4 = isone ? 0 : 4096;
    for (int pass = 0;; ++pass) {
        unsigned kpa = c.lds0 + LDS_K + kh * 32768 + c.l16; asm volatile("" : "+v"(kpa));
        unsigned vpa = c.lds0 + (isone ? LDS_ONES + (h2 ? 0 : 128) + inrow : LDS_V + kh * 32768 + h2 * 128 + inrow); asm volatile("" : "+v"(vpa));
        f32x16 oA = {}, oB = {};
#define KRD(t) asbf(lds16(kpa + (t) * 1024))
#define VTR(off) ldstr(vpa + (off))
        WAITV_BAR(4);
        TS(2);
        if (pass == 0) {
            const bf16x8 k0 = asbf(lds16(c.lds0 + LDS_K + c.l16)); const f32x16 zero = {};
            const f32x16 a = MFMA(k0, qA, zero), bq = MFMA(k0, qB, zero);
            float m0 = fmaxf(a[0], bq[0]), m1 = fmaxf(a[1], bq[1]);
#pragma unroll
            for (int i = 2; i < 16; i += 2) { m0 = fmaxf(fmaxf(m0, a[i]), bq[i]); m1 = fmaxf(fmaxf(m1, a[i + 1]), bq[i + 1]); }
            mref = swap32_max(fmaxf(m0, m1));
        }
        f32x16 nm;
#pragma unroll
        for (int i = 0; i < 16; ++i) nm[i] = -mref;
        asm volatile("" : "+v"(nm));
        bf16x8 kf = KRD(0);
        s16x4 va0 = VTR(0), va1 = VTR(256), vb0 = VTR(512), vb1 = VTR(768);
        f32x16 sA = MFMA(kf, qA, nm), sB;
        u32x4 p0, p1;
#define EX(cc, i) do { cc[i] = __builtin_amdgcn_exp2f(cc[i]); SBAR(); } while (0)
#define CV(p, jj, cc, i) do { p[jj] = cvtpk(cc[i], cc[i + 1]); SBAR(); } while (0)
#define STREAM(cur, ocur, prv, oprv, qprv, nmprv, tl, ISA, PEND) do { \
        SBAR(); \
        EX(cur, 0); EX(cur, 1); if (PEND) CV(p1, 2, prv, 12); EX(cur, 2); if (PEND) CV(p1, 3, prv, 14); EX(cur, 3); EX(cur, 4); \
        if (PEND) { oprv = MFMA(cat(vb0, vb1), asbf(p1), oprv); asm volatile("" : "+v"(oprv)); SBAR(); } \
        if (ISA && PEND) { vb0 = VTR((tl) * 1024 + 512); vb1 = VTR((tl) * 1024 + 768); SBAR(); } \
        CV(p0, 0, cur, 0); EX(cur, 5); EX(cur, 6); CV(p0, 1, cur, 2); EX(cur, 7); EX(cur, 8); \
        prv = MFMA(kf, qprv, nmprv); asm volatile("" : "+v"(prv)); SBAR(); \
        if (ISA) { kf = KRD((tl) + 1); SBAR(); } \
        CV(p0, 2, cur, 4); EX(cur, 9); EX(cur, 10); CV(p0, 3, cur, 6); EX(cur, 11); EX(cur, 12); \
        ocur = MFMA(cat(va0, va1), asbf(p0), ocur); asm volatile("" : "+v"(ocur)); SBAR(); \
        if (!(ISA)) { va0 = VTR(((tl) + 1) * 1024); va1 = VTR(((tl) + 1) * 1024 + 256); SBAR(); } \
        CV(p1, 0, cur, 8); EX(cur, 13); EX(cur, 14); CV(p1, 1, cur, 10); EX(cur, 15); \
    } while (0)
#define PAIR(tl, PEND) do { STREAM(sA, oA, sB, oB, qB, nm, tl, true, PEND); STREAM(sB, oB, sA, oA, qA, nm, tl, false, true); } while (0)
#define BUMP(n, vs) do { kpa += (n) * 1024; vpa += (vs); asm volatile("" : "+v"(kpa), "+v"(vpa)); } while (0)
        PAIR(0, false);
        PAIR(1, true);
        PAIR(2, true);
        WAITV_BAR(0);
        if (pass == 0) {
            int w_ = wid, b_ = bb, l_ = l; OPAQUE_S(w_); OPAQUE_S(b_); OPAQUE_S(l_);
            const int qp_ = w_ & 3, kh_ = w_ >> 2;
            const unsigned sk = c.lds0 + LDS_K + kh_ * 32768 + (12 + 5 * qp_) * 1024, i0 = 8 * (b_ >> 5) + 16 + 28 * kh_ + 5 * qp_;
            const unsigned kb_ = OFF_K + l_ * ACT_B + ((b_ & 7) + 8 * ((b_ >> 3) & 3)) * 65536;
#pragma unroll
            for (int i = 0; i < 5; ++i) { const unsigned tC = (i0 + i) & 63;
                dma<1>(c.rs, c.l16, kb_ + tC * 1024, sk + i * 1024);
                dma<1>(c.rs, c.l16, kb_ + (OFF_V - OFF_K) + tC * 1024, sk + (LDS_V - LDS_K) + i * 1024); }
        }
        PAIR(3, true);
        BUMP(4, vstep4);
#pragma unroll
        for (int u = 0; u < ATT_UNR; ++u) {
            PAIR(u, true); }
        BUMP(ATT_UNR, vstep);
        WAITV_BAR(0);
        if (l == 0) await<4>(c, PH_PREP, lane);
        STAMP(4 * (2 + 5 * l) + 0); TS(3);
        if (l == 0) WSTAMP(64);
        int w_ = wid, j_ = j, s_ = s; OPAQUE_S(w_); OPAQUE_S(j_); OPAQUE_S(s_);
        const unsigned o1 = OFF_WO + (2 * j_ + (w_ & 1)) * 32 * 1024, o2 = w1_off(w_, j_); const int rot_ = rot32(s_, j_), q4_ = 4 * (w_ >> 1);
#define PF(i) do { if ((i) < 8) wo[(i) & 7] = ld16(c.rs, c.l16, fr32(o1, 16 * (((i) & 7) >> 2) + ((i) & 3) + q4_, rot_)); \
        else if ((i) < 8 + W1_PRE) wf[((i) - 8) & 31] = ld16(c.rs, c.l16, fr32(o2, (i) - 8, rot_)); SBAR(); } while (0)
        if (w_ >= 4) __builtin_amdgcn_s_setprio(1);
#pragma unroll
        for (int blk = 0; blk < 3; ++blk) {
#pragma unroll
            for (int u = 0; u < ATT_UNR; ++u) {
                if (ATT_UNR * blk + u == ATT_PRIO_END) __builtin_amdgcn_s_setprio(0);
                STREAM(sA, oA, sB, oB, qB, nm, u, true, true);  PF(2 * (ATT_UNR * blk + u));
                STREAM(sB, oB, sA, oA, qA, nm, u, false, true); PF(2 * (ATT_UNR * blk + u) + 1);
            }
            BUMP(ATT_UNR, vstep);
        }
#undef PF
        CV(p1, 2, sB, 12); CV(p1, 3, sB, 14);
        oB = MFMA(cat(vb0, vb1), asbf(p1), oB);
        STAMP(4 * (2 + 5 * l) + 2); TS(4);
        if (l == 0) WSTAMP(56);
        {
            const f32x16 give = kh ? oA : oB;
            const unsigned xb = c.lds0 + LDS_XCH + wid * 2304;
            *(LAS f32x4*)(uintptr_t)(xb + c.l16) = (f32x4){give[0], give[1], give[2], give[3]};
            *(LAS f32x4*)(uintptr_t)(xb + 1024 + c.l16) = (f32x4){give[4], give[5], give[6], give[7]};
            *(LAS float*)(uintptr_t)(xb + 2048 + lane * 4) = give[8];
            if (wid == 0 && lane == 0) *(LAS unsigned*)(uintptr_t)(c.lds0 + LDS_FLAG) = 0u;
        }
        BAR();
        f32x16 o = kh ? oB : oA;
        {
            const unsigned xb = c.lds0 + LDS_XCH + (wid ^ 4) * 2304;
            const f32x4 u0 = ldsf4(xb + c.l16), u1 = ldsf4(xb + 1024 + c.l16);
            const float ul = *(const LAS float*)(uintptr_t)(xb + 2048 + lane * 4);
#pragma unroll
            for (int i = 0; i < 4; ++i) { o[i] += u0[i]; o[4 + i] += u1[i]; }
            o[8] = swap32_lo(o[8] + ul);
        }
        const float rl = __builtin_amdgcn_rcpf(o[8]);
        int w2_ = wid, b2_ = bb, l2_ = l; OPAQUE_S(w2_); OPAQUE_S(b2_); OPAQUE_S(l2_);
        const unsigned o_off = OFF_O + l2_ * ACT_B + ((8 * (b2_ >> 5) + 2 * (w2_ & 3) + (w2_ >> 2)) * 32 + (b2_ & 7) + 8 * ((b2_ >> 3) & 3)) * 1024;
        st16(c.rs, (u32x4){cvtpk(o[0] * rl, o[1] * rl), cvtpk(o[2] * rl, o[3] * rl), cvtpk(o[4] * rl, o[5] * rl), cvtpk(o[6] * rl, o[7] * rl)}, c.l16,
             pass == 0 || pass == 1 + (w2_ >> 2) ? o_off : OOB);
        const float omax = fmaxf(fmaxf(fmaxf(fabsf(o[0]), fabsf(o[1])), fmaxf(fabsf(o[2]), fabsf(o[3]))), fmaxf(fmaxf(fabsf(o[4]), fabsf(o[5])), fmaxf(fabsf(o[6]), fabsf(o[7]))));
        const bool bad = !(o[8] >= 8.673617379884035e-19f   && o[8] <= 1.152921504606847e18f   && omax <= 3.0e38f);
        if (pass == 0 && __any(bad) && lane == 0) *(LAS unsigned*)(uintptr_t)(c.lds0 + LDS_FLAG) = 1u;
        WAITV_BAR(0);
        TS(7);
        if (pass == 2 || (pass == 0 && __builtin_amdgcn_readfirstlane(*(const LAS unsigned*)(uintptr_t)(c.lds0 + LDS_FLAG)) == 0u)) break;
        if (pass == 0) {
            float mA = -3.0e38f; mB = -3.0e38f;
            const f32x16 zero = {};
            for (int t = 0; t < 64; ++t) {
                const bf16x8 kk = asbf(lds16(c.lds0 + LDS_K + t * 1024 + c.l16));
                const f32x16 a = MFMA(kk, qA, zero), bq = MFMA(kk, qB, zero);
#pragma unroll
                for (int i = 0; i < 16; ++i) { mA = fmaxf(mA, a[i]); mB = fmaxf(mB, bq[i]); }
            }
            mref = swap32_max(mA); mB = swap32_max(mB);
        } else mref = mB;
        BAR();
#undef KRD
#undef VTR
#undef EX
#undef CV
#undef STREAM
#undef PAIR
#undef BUMP
    }
    TSFLUSH(l == 0 ? 1 : 6);
    if (c.wid == 0 && c.lane == 0) { u64 t = c.tag; asm volatile("" : "+s"(t)); t += (u64)(PH_ATT + lp);
        st8(c.rs, (u32x2){(unsigned)t, (unsigned)(t >> 32)}, 0, OFF_FLAGS + ((PH_ATT + lp) * 256 + c.b) * 128); }
    asm volatile("" ::: "memory");
    {
        int w_ = wid, j_ = j, s_ = s; OPAQUE_S(w_); OPAQUE_S(j_); OPAQUE_S(s_);
        const unsigned o2 = w1_off(w_, j_); const int rot_ = rot32(s_, j_);
#pragma unroll
        for (int i = W1_PRE; i < W1_PRE + W1_MID; ++i) wf[i] = ld16(c.rs, c.l16, fr32(o2, i, rot_));
    }
}

DI void res_finish(const Ctx& c, const Params& p, int w, int s, int j, bool last, const f32x16& v, unsigned x_off, unsigned st_off, unsigned (&xres)[8]) {
    const int lane = c.lane, tb = w >> 1, nb = w & 1;
    f32x2 y[8];
#pragma unroll
    for (int m = 0; m < 8; ++m) { y[m] = (f32x2){bflo(xres[m]), bfhi(xres[m])} + (f32x2){v[2 * m], v[2 * m + 1]}; xres[m] = cvtpk(y[m][0], y[m][1]); }
    if (last) {
        const rsrc_t ro = mkrsrc(p.out, 2048 * 512 * 2);
        const unsigned vo = (64 * s + 32 * tb + (lane & 31)) * 1024 + (64 * j + 32 * nb) * 2 + (lane >> 5) * 16;
#pragma unroll
        for (int k = 0; k < 2; ++k) {
            const auto a = __builtin_amdgcn_permlane32_swap(xres[4 * k], xres[4 * k + 2], false, false);
            const auto b = __builtin_amdgcn_permlane32_swap(xres[4 * k + 1], xres[4 * k + 3], false, false);
            __builtin_amdgcn_raw_buffer_store_b128((u32x4){a[0], b[0], a[1], b[1]}, ro, vo + 32 * k, 0, 0);
        }
        return;
    }
    const unsigned base = x_off + ((2 * s + tb) * 32 + 4 * j + 2 * nb) * 1024;
    st16(c.rs, (u32x4){xres[0], xres[1], xres[2], xres[3]}, c.l16, base);
    st16(c.rs, (u32x4){xres[4], xres[5], xres[6], xres[7]}, c.l16, base + 1024);
    f32x2 s2 = (y[0] + y[1]) + (y[2] + y[3]), q2 = y[0] * y[0];
    s2 += (y[4] + y[5]) + (y[6] + y[7]);
#pragma unroll
    for (int m = 1; m < 8; ++m) q2 += y[m] * y[m];
    const float sm = swap32_sum(s2[0] + s2[1]), sq = swap32_sum(q2[0] + q2[1]);
    if (lane < 32) st8(c.rs, (u32x2){__float_as_uint(sm), __float_as_uint(sq)}, lane * 128, st_off + (64 * s + 32 * tb) * 128 + (2 * j + nb) * 8);
}
DI void put16(unsigned a, const f32x16& v) {
#pragma unroll
    for (int m = 0; m < 4; ++m) *(LAS f32x4*)(uintptr_t)(a + m * 1024) = (f32x4){v[4 * m], v[4 * m + 1], v[4 * m + 2], v[4 * m + 3]};
}
DI void add16x3(unsigned a, f32x16& v) {
    f32x4 t[12];
#pragma unroll
    for (int m = 0; m < 12; ++m) t[m] = ldsf4(a + m * 1024);
#pragma unroll
    for (int m = 0; m < 4; ++m) { const f32x4 u = (t[m] + t[4 + m]) + t[8 + m]; v[4 * m] += u[0]; v[4 * m + 1] += u[1]; v[4 * m + 2] += u[2]; v[4 * m + 3] += u[3]; }
}

DI void parts_out(unsigned base, int nb, int q, int tbA, const f32x16& acc0, const f32x16& acc1) {
    const int sl = q > 0 ? q - 1 : 0;
    put16(base + ((nb + 2 * (1 - tbA)) * 3 + sl) * 4096, acc1);
    if (q >= 2) put16(base + (nb * 3 + sl) * 4096, acc0);
}
DI void phase_wo(const Ctx& c0, const Params& p, int l, int lp, int s, int j, unsigned (&xres)[8], u32x4 (&wo)[8], u32x4 (&wf)[32]) {
    OPAQUE_S(lp); OPAQUE_S(l); const Ctx c = fresh(c0); const int w = c.wid; OPAQUE_S(s); OPAQUE_S(j);
    const int nb = w & 1, q = w >> 1, tbA = q == 1 ? 1 : 0;
    if (w < 4) {
        const unsigned o = OFF_XT + ((2 * s + (w >> 1)) * 32 + 4 * j + 2 * nb) * 1024;
        const u32x4 a = ld16(c.rs, c.l16, o), b = ld16(c.rs, c.l16, o + 1024);
#pragma unroll
        for (int m = 0; m < 4; ++m) { xres[m] = a[m]; xres[4 + m] = b[m]; }
    }
    TSDECL; TS(0);
    await<1>(c, PH_ATT + lp, blk_attn(c.lane & 31, s >> 2));
    STAMP(4 * (3 + 5 * l) + 1); TS(1);
    const unsigned a_off = OFF_O + l * ACT_B + 2 * s * 32 * 1024;
    const int rot = rot32(s, j);
#define ISSUE(n) do { const unsigned o_ = 16 * ((n) >> 1) + 4 * (w & 3) + 2 * ((n) & 1); \
        dma<2>(c.rs, c.l16, fr32(a_off + (w >> 2) * 32768, o_, rot), c.lds0 + (w >> 2) * 32768 + o_ * 1024); } while (0)
    ISSUE(0); ORDER(); ISSUE(1); ISSUE(2); ISSUE(3); ORDER();
#undef ISSUE
    const unsigned nxt = w1_off(w, j);
    f32x16 acc0 = {}, acc1 = {};
    const unsigned ab0 = c.lds0 + c.l16 + q * 4096 + tbA * 32768, ab1 = c.lds0 + c.l16 + q * 4096 + (1 - tbA) * 32768;
#define COMPUTE(n) do { const unsigned o_ = (16 * ((n) >> 1) + 2 * ((n) & 1)) * 1024; u32x4 A0[2], A1[2]; \
        _Pragma("unroll") for (int i = 0; i < 2; ++i) { A0[i] = lds16(ab0 + o_ + i * 1024); A1[i] = lds16(ab1 + o_ + i * 1024); } SBAR(); \
        _Pragma("unroll") for (int i = 0; i < 2; ++i) { \
            acc0 = MFMA(asbf(wo[2 * (n) + i]), asbf(A0[i]), acc0); acc1 = MFMA(asbf(wo[2 * (n) + i]), asbf(A1[i]), acc1); SBAR(); } } while (0)
    WAITV_BAR(6); TS(2); COMPUTE(0);
    WAITV_BAR(0); COMPUTE(1); COMPUTE(2); COMPUTE(3);
#undef COMPUTE
    STAMP(4 * (3 + 5 * l) + 2); TS(3);
    parts_out(c.lds0 + 65536 + c.l16, nb, q, tbA, acc0, acc1);
    BAR();
    TS(4);
    if (w < 4) {
        const unsigned xa = c.lds0 + 65536 + w * 3 * 4096 + c.l16;
        add16x3(xa, acc0);
        res_finish(c, p, w, s, j, false, acc0, OFF_X1 + l * ACT_B, OFF_ST + (2 * l) * ST_B, xres);
    }
    TS(5);
    publish<0>(c, PH_WO + lp);
    TS(6);
#pragma unroll
    for (int i = W1_PRE + W1_MID; i < 32; ++i) wf[i] = ld16(c.rs, c.l16, fr32(nxt, i, rot));
    TS(7); if (l == 0) TSFLUSH(2);
}

#define W2MID 12
constexpr int W2_MID = W2MID;
DI unsigned w2_off(int w, int j) { return OFF_W2 + ((2 * j + (w & 1)) * 112 + 4 * (w >> 1)) * 1024; }
DI void phase_f1(const Ctx& c0, int l, int lp, int s, int j, u32x4 (&wf)[32]) {
    OPAQUE_S(lp); OPAQUE_S(l); const Ctx c = fresh(c0); const int w = c.wid; OPAQUE_S(s); OPAQUE_S(j);
    const int nb = 7 * j + w;
    f32x16 cs; load_cs(c, w < 7 ? OFF_CS1 + nb * 128 : OOB, cs);
    TSDECL; TS(0);
    await<1>(c, PH_WO + lp, blk_chain(s, c.lane & 7));
    TS(1);
    STAMP(4 * (4 + 5 * l) + 1);
    if (l == 0) XST(79);
    const int rot = rot32(s, j), r7 = rot7(s, j);
    issue_a512(c, w, OFF_X1 + l * ACT_B + 2 * s * 32 * 1024, OFF_ST + (2 * l) * ST_B + s * 8192, rot);
    constexpr int LATE1 = TRAIL ? 0 : 32 - W1_PRE - W1_MID;
    const unsigned ab = c.lds0 + c.l16, nxt = w2_off(w, j);
#define W2FRAG(i) ld16(c.rs, c.l16, nxt + (16 * chunk7((i) >> 2, r7) + ((i) & 3)) * 1024)
#define F1_WAIT(n) do { WAITV_BAR(((n) == 0 ? 6 : 0) + LATE1); if (l == 0) XST(80 + (n)); if ((n) == 0) TS(2); if ((n) == 1) TS(3); } while (0)
#define F1_OUT(tb) do { float z[16]; \
        _Pragma("unroll") for (int i = 0; i < 16; ++i) z[i] = (acc[i] - mu * cs[i]) * rs_; \
        const unsigned base = w < 7 ? OFF_H + l * H_B + ((2 * s + (tb)) * 112 + 2 * nb) * 1024 : OOB; \
        st16(c.rs, (u32x4){relupk(z[0], z[1]), relupk(z[2], z[3]), relupk(z[4], z[5]), relupk(z[6], z[7])}, c.l16, base); \
        st16(c.rs, (u32x4){relupk(z[8], z[9]), relupk(z[10], z[11]), relupk(z[12], z[13]), relupk(z[14], z[15])}, c.l16, base + 1024); } while (0)
    {
        f32x16 acc = {}; float mu, rs_; RowStat st;
#define F1_AFTER(ks) do { if ((ks) == 8) stat_read(c, 0, st); if ((ks) == 13) stat_finish(st, mu, rs_); } while (0)
        KLOOP1(ab, wf, acc, F1_WAIT, F1_AFTER);
#undef F1_AFTER
        F1_OUT(0);
    }
    {
        f32x16 acc = {}; float mu, rs_; RowStat st;
#define F1_AFTER(ks) do { if ((ks) < W2_MID) wf[ks] = W2FRAG(ks); if ((ks) == 22) stat_read(c, 1, st); if ((ks) == 27) stat_finish(st, mu, rs_); } while (0)
        KLOOP1(ab + 32768, wf, acc, NOTHING, F1_AFTER);
#undef F1_AFTER
        TS(4);
        STAMP(4 * (4 + 5 * l) + 2);
        F1_OUT(1);
    }
#undef F1_WAIT
#undef F1_OUT
    TS(5);
    publish<0>(c, PH_F1 + lp);
    TS(6);
#pragma unroll
    for (int i = W2_MID; i < 28; ++i) wf[i] = W2FRAG(i);
#undef W2FRAG
    TS(7); if (l == 0) TSFLUSH(3);
}

DI void phase_f2(const Ctx& c0, const Params& p, int l, int lp, int s, int j, unsigned (&xres)[8], u32x4 (&wf)[32]) {
    OPAQUE_S(lp); OPAQUE_S(l); const Ctx c = fresh(c0); const int w = c.wid; OPAQUE_S(s); OPAQUE_S(j);
    const int nb = w & 1, q = w >> 1, tbA = q == 1 ? 1 : 0;
    const bool last = l != 0;
    TSDECL; TS(0);
    await<1>(c, PH_F1 + lp, blk_chain(s, c.lane & 7));
    STAMP(4 * (5 + 5 * l) + 1); TS(1);
    const unsigned a_off = OFF_H + l * H_B + 2 * s * 112 * 1024;
    const int r7 = rot7(s, j), rot = rot32(s, j);
#define ISSUE(ch) dma<4>(c.rs, c.l16, a_off + ((w >> 2) * 112 + 16 * chunk7(ch, r7) + 4 * (w & 3)) * 1024, c.lds0 + ((ch) & 3) * 32768 + (w >> 2) * 16384 + 4 * (w & 3) * 1024)
    f32x16 acc0 = {}, acc1 = {};
    const unsigned ab0 = c.lds0 + c.l16 + q * 4096 + tbA * 16384, ab1 = c.lds0 + c.l16 + q * 4096 + (1 - tbA) * 16384;
#define COMPUTE(ch) do { const unsigned o_ = ((ch) & 3) * 32768; u32x4 A0[4], A1[4]; \
        _Pragma("unroll") for (int i = 0; i < 2; ++i) { A0[i] = lds16(ab0 + o_ + i * 1024); A1[i] = lds16(ab1 + o_ + i * 1024); } SBAR(); \
        _Pragma("unroll") for (int i = 0; i < 4; ++i) { \
            if (i < 2) { A0[i + 2] = lds16(ab0 + o_ + (i + 2) * 1024); A1[i + 2] = lds16(ab1 + o_ + (i + 2) * 1024); } SBAR(); \
            acc0 = MFMA(asbf(wf[4 * (ch) + i]), asbf(A0[i]), acc0); acc1 = MFMA(asbf(wf[4 * (ch) + i]), asbf(A1[i]), acc1); SBAR(); } } while (0)
    const unsigned nxt = wq_off(w, j);
    constexpr int P = QKV_P;
    ISSUE(0); ORDER(); ISSUE(1); ORDER(); ISSUE(2); ISSUE(3); ORDER();
    constexpr int R = TRAIL ? 0 : 28 - W2_MID;
    WAITV_BAR(12 + R); TS(2); COMPUTE(0);
    WAITV_BAR(8 + R); ISSUE(4); COMPUTE(1);
    WAITV_BAR(4 + R + 4); ISSUE(5); COMPUTE(2);
    WAITV_BAR(R + 8); ISSUE(6); COMPUTE(3);
    WAITV_BAR(8); COMPUTE(4);
    WAITV_BAR(4); COMPUTE(5);
    WAITV_BAR(0); TS(3); COMPUTE(6);
#undef ISSUE
#undef COMPUTE
    STAMP(4 * (5 + 5 * l) + 2);
    parts_out(c.lds0 + c.l16, nb, q, tbA, acc0, acc1);
    BAR();
    TS(4);
    if (w < 4) {
        const unsigned xa = c.lds0 + w * 3 * 4096 + c.l16;
        add16x3(xa, acc0);
        res_finish(c, p, w, s, j, last, acc0, OFF_XT, OFF_ST + ST_B, xres);
    }
    if (last) { __builtin_amdgcn_s_waitcnt(waitcnt_imm(0, 15)); return; }
    asm volatile("" ::: "memory");
#pragma unroll
    for (int i = 0; i < P; ++i) wf[i] = ld16(c.rs, c.l16, fr32(nxt, i, rot));
    TS(5);
    publish<P>(c, PH_F2);
    TS(6);
#pragma unroll
    for (int i = P; i < QKV_PRE; ++i) wf[i] = ld16(c.rs, c.l16, fr32(nxt, i, rot));
    TS(7); TSFLUSH(4);
}

__global__ void __launch_bounds__(NT, 2) fused_layers_kernel(Params p) {
    extern __shared__ __attribute__((aligned(16))) char lds[];
    Ctx c;
    c.ws = (GAS char*)p.ws; c.rs = mkrsrc(p.ws, WS_BYTES);
    c.lds0 = (unsigned)(uintptr_t)(LAS char*)lds;
    c.lane = threadIdx.x & 63; c.wid = __builtin_amdgcn_readfirstlane(threadIdx.x >> 6); c.l16 = c.lane * 16; c.b = blockIdx.x;
    c.tag = 0;
    const int x = c.b & 7, r = c.b >> 3, s = 4 * x + (r >> 3), j = r & 7;
    {
        u64 pc; asm volatile("s_getpc_b64 %0" : "=s"(pc));
        if (c.wid == 7) warm4k(mkrsrc((const void*)(pc & ~(u64)63), 37 * 1024), r < 10 ? r * 4096 + c.lane * 64 : OOB, 0, c.lds0 + LDS_DUMMY);
    }
    unsigned xres[8];
    u32x4 wf[32], wo[8];
    ESTAMP(0);
    phase_first(c, p, s, j);
    ESTAMP(7);
#pragma unroll 1
    for (int l_ = 0;; ++l_) {
        int l = __builtin_amdgcn_readfirstlane(l_); OPAQUE_S(l);
        int lp = PH_PER_LAYER * l; OPAQUE_S(lp);
        phase_attn(c, l, lp, s, j, wf, wo);
        ESTAMP(4 * (2 + 5 * l) + 3);
        phase_wo(c, p, l, lp, s, j, xres, wo, wf);
        ESTAMP(4 * (3 + 5 * l) + 3);
        phase_f1(c, l, lp, s, j, wf);
        ESTAMP(4 * (4 + 5 * l) + 3);
        phase_f2(c, p, l, lp, s, j, xres, wf);
        ESTAMP(4 * (5 + 5 * l) + 3);
        if (l != 0) break;
        phase_qkv1(c, s, j, wf);
        ESTAMP(4 * 6 + 3);
    }
    const Ctx ce = fresh(c);
    if (ce.b == 0 && ce.wid == 0 && ce.lane == 0) { const u64 t = ce.tag + 15; st8(ce.rs, (u32x2){(unsigned)t, (unsigned)(t >> 32)}, 0, OFF_EPOCH); }
}

}

extern "C" void kernel_launch(void* const* d_in, const int* in_sizes, int n_in, void* d_out, int out_size, void* d_ws, size_t ws_size, hipStream_t stream) {
    (void)in_sizes; (void)n_in; (void)out_size; (void)ws_size;
    Params p;
    p.x = d_in[0]; p.wqkv = d_in[1]; p.wo = d_in[2]; p.w1 = d_in[4]; p.w2 = d_in[5]; p.out = d_out; p.ws = d_ws;
    (void)hipFuncSetAttribute((const void*)fused_layers_kernel, hipFuncAttributeMaxDynamicSharedMemorySize, LDS_TOTAL);
    hipLaunchKernelGGL(fused_layers_kernel, dim3(256), dim3(NT), LDS_TOTAL, stream, p);
}
```

```cpp
#include <hip/hip_runtime.h>
#include <hip/hip_bf16.h>
#include <stdint.h>

namespace {

#define LAS __attribute__((address_space(3)))
#define DI __device__ __forceinline__
typedef __bf16 bf16x8 __attribute__((ext_vector_type(8)));
typedef __bf16 bf16x2 __attribute__((ext_vector_type(2)));
typedef short s16x4 __attribute__((ext_vector_type(4)));
typedef float f32x2 __attribute__((ext_vector_type(2)));
typedef float f32x4 __attribute__((ext_vector_type(4)));
typedef float f32x16 __attribute__((ext_vector_type(16)));
typedef unsigned u32x2 __attribute__((ext_vector_type(2)));
typedef unsigned u32x4 __attribute__((ext_vector_type(4)));
typedef unsigned long long u64;
typedef __amdgpu_buffer_rsrc_t rsrc_t;

constexpr int NT = 512;
constexpr unsigned OFF_EPOCH = 0;
constexpr unsigned OFF_FLAGS = 0x1000;
constexpr unsigned OFF_CSQ   = 0x70000;
constexpr unsigned OFF_CS1   = 0x72000;
constexpr unsigned OFF_ST    = 0x2C00000;
constexpr unsigned OFF_WQ    = 0x100000;
constexpr unsigned OFF_WO    = 0x280000;
constexpr unsigned OFF_W1    = 0x300000;
constexpr unsigned OFF_W2    = 0x4C0000;
constexpr unsigned OFF_XT    = 0x700000;
constexpr unsigned OFF_Q     = 0xA00000;
constexpr unsigned OFF_K     = 0xE00000;
constexpr unsigned OFF_V     = 0x1200000;
constexpr unsigned OFF_O     = 0x1600000;
constexpr unsigned OFF_X1    = 0x1A00000;
constexpr unsigned OFF_H     = 0x1E00000;
constexpr unsigned WS_BYTES  = 0x2CC0000;
constexpr unsigned ACT_B = 0x200000, H_B = 0x700000, ST_B = 0x40000;
constexpr int PH_PREP = 0, PH_QKV = 1, PH_ATT = 2, PH_WO = 3, PH_F1 = 4, PH_F2 = 5, PH_PER_LAYER = 5;
constexpr int ATT_UNR = 7;
constexpr int LDS_K = 0, LDS_V = 65536, LDS_ONES = 131072, ONES_BYTES = (ATT_UNR + 2) * 1024 + 256;
constexpr int LDS_XCH = LDS_ONES + ONES_BYTES;
constexpr int LDS_FLAG = LDS_XCH + 8 * 2304;
constexpr int LDS_DUMMY = LDS_FLAG + 64;
constexpr int LDS_TOTAL = LDS_DUMMY + 256;
static_assert(LDS_TOTAL <= 163840, "LDS");

struct Params { const void* x; const void* wqkv; const void* wo; const void* w1; const void* w2; void* out; void* ws; };

#define MFMA(a, b, c) __builtin_amdgcn_mfma_f32_32x32x16_bf16((a), (b), (c), 0, 0, 0)
#define SBAR() __builtin_amdgcn_sched_barrier(0)
#define BAR() asm volatile("s_waitcnt lgkmcnt(0)\n\ts_barrier" ::: "memory")
constexpr int waitcnt_imm(int vm, int lgkm) { return (vm & 15) | ((vm >> 4) << 14) | (7 << 4) | (lgkm << 8); }
#define WAITV_BAR(n) do { asm volatile("" ::: "memory"); __builtin_amdgcn_s_waitcnt(waitcnt_imm(n, 0)); asm volatile("s_barrier" ::: "memory"); } while (0)
#define ORDER() asm volatile("s_barrier" ::: "memory")

DI unsigned cvtpk(float lo, float hi) { f32x2 v = {lo, hi}; return __builtin_bit_cast(unsigned, __builtin_convertvector(v, bf16x2)); }
DI unsigned relupk(float lo, float hi) { typedef short s16x2 __attribute__((ext_vector_type(2)));
    const s16x2 v = __builtin_bit_cast(s16x2, cvtpk(lo, hi)); return __builtin_bit_cast(unsigned, __builtin_elementwise_max(v, (s16x2){0, 0})); }
DI bf16x8 asbf(u32x4 v) { return __builtin_bit_cast(bf16x8, v); }
DI float bflo(unsigned u) { return __uint_as_float(u << 16); }
DI float bfhi(unsigned u) { return __uint_as_float(u & 0xffff0000u); }
DI rsrc_t mkrsrc(const void* p, unsigned bytes) { return __builtin_amdgcn_make_buffer_rsrc((void*)p, 0, bytes, 0x00020000); }
DI u32x4 ld16(rsrc_t rs, unsigned voff, unsigned soff) { return __builtin_amdgcn_raw_buffer_load_b128(rs, voff, soff, 16); }
DI void st16(rsrc_t rs, u32x4 v, unsigned voff, unsigned soff) { __builtin_amdgcn_raw_buffer_store_b128(v, rs, voff + soff, 0, 16); }
DI void st8(rsrc_t rs, u32x2 v, unsigned voff, unsigned soff) { __builtin_amdgcn_raw_buffer_store_b64(v, rs, voff, soff, 16); }
DI void st4(rsrc_t rs, unsigned v, unsigned voff, unsigned soff) { __builtin_amdgcn_raw_buffer_store_b32(v, rs, voff, soff, 16); }
#define DMA_HEAD "s_nop 2\n\ts_mov_b32 m0, %0\n\ts_nop 0\n\t"
#define DMA_ONE(off, pol) "buffer_load_dwordx4 %1, %2, %3 offen offset:" #off pol " lds\n\t"
template <int N> DI void dma(rsrc_t rs, unsigned voff, unsigned soff, unsigned ldsdst) {
    if constexpr (N == 1) asm volatile(DMA_HEAD DMA_ONE(0, " sc1") :: "s"(ldsdst), "v"(voff), "s"(rs), "s"(soff) : "memory");
    if constexpr (N == 2) asm volatile(DMA_HEAD DMA_ONE(0, " sc1") DMA_ONE(1024, " sc1") :: "s"(ldsdst), "v"(voff), "s"(rs), "s"(soff) : "memory");
    if constexpr (N == 4) asm volatile(DMA_HEAD DMA_ONE(0, " sc1") DMA_ONE(1024, " sc1") DMA_ONE(2048, " sc1") DMA_ONE(3072, " sc1") :: "s"(ldsdst), "v"(voff), "s"(rs), "s"(soff) : "memory");
}
DI void dma_in(rsrc_t rs, unsigned voff, unsigned soff, unsigned ldsdst) {
    asm volatile(DMA_HEAD DMA_ONE(0, "") :: "s"(ldsdst), "v"(voff), "s"(rs), "s"(soff) : "memory");
}
DI void warm4k(rsrc_t rs, unsigned l64, unsigned soff, unsigned ldsdst) {
    asm volatile("s_nop 2\n\ts_mov_b32 m0, %0\n\ts_nop 0\n\tbuffer_load_dword %1, %2, %3 offen lds" :: "s"(ldsdst), "v"(l64), "s"(rs), "s"(soff) : "memory");
}
#define OPAQUE_S(x) asm volatile("" : "+s"(x))
DI u32x4 lds16(unsigned a) { return *(const LAS u32x4*)(uintptr_t)a; }
DI f32x4 ldsf4(unsigned a) { return *(const LAS f32x4*)(uintptr_t)a; }
DI s16x4 ldstr(unsigned a) { return __builtin_amdgcn_ds_read_tr16_b64_v4i16((LAS s16x4*)(uintptr_t)a); }
DI bf16x8 cat(s16x4 a, s16x4 b) { return __builtin_bit_cast(bf16x8, __builtin_shufflevector(a, b, 0, 1, 2, 3, 4, 5, 6, 7)); }
template <int CTRL> DI float dpp(float x) { return __builtin_bit_cast(float, __builtin_amdgcn_mov_dpp(__builtin_bit_cast(int, x), CTRL, 0xf, 0xf, true)); }
DI float swap32_sum(float x) { auto r = __builtin_amdgcn_permlane32_swap(__float_as_uint(x), __float_as_uint(x), false, false); return __uint_as_float(r[0]) + __uint_as_float(r[1]); }
DI float swap32_max(float x) { auto r = __builtin_amdgcn_permlane32_swap(__float_as_uint(x), __float_as_uint(x), false, false); return fmaxf(__uint_as_float(r[0]), __uint_as_float(r[1])); }
DI float swap32_lo(float x) { auto r = __builtin_amdgcn_permlane32_swap(__float_as_uint(x), __float_as_uint(x), false, false); return __uint_as_float(r[0]); }
DI float sum8(float x) { x += dpp<0xB1>(x); x += dpp<0x4E>(x); x += dpp<0x141>(x); return x; }

struct Ctx {
    rsrc_t rs;
    u64 tag;
    unsigned lds0, l16;
    int lane, wid, b;
};
DI Ctx fresh(const Ctx& c0) {
    Ctx c = c0;
    int lane = __builtin_amdgcn_mbcnt_hi(~0u, __builtin_amdgcn_mbcnt_lo(~0u, 0u));
    asm volatile("" : "+v"(lane));
    c.lane = lane; c.l16 = lane * 16;
    asm volatile("" : "+s"(c.wid), "+s"(c.b), "+s"(c.lds0));
    return c;
}
DI int blk_chain(int s, int j) { return (s >> 2) + 8 * ((s & 3) * 8 + j); }
DI int blk_attn(int h, int g) { return (h & 7) + 8 * (4 * g + (h >> 3)); }
DI unsigned frag(unsigned base, int n) { return base + n * 1024; }

template <int N> DI void publish(const Ctx& c, int phase) {
    WAITV_BAR(N);
    if (c.wid == 0 && c.lane == 0) { u64 t = c.tag; asm volatile("" : "+s"(t)); t += (u64)phase; st8(c.rs, (u32x2){(unsigned)t, (unsigned)(t >> 32)}, 0, OFF_FLAGS + (phase * 256 + c.b) * 128); }
}
template <int PER_LANE> DI void await(const Ctx& c, int phase, int pb) {
    if (c.wid == 0) {
        u64 want = c.tag; asm volatile("" : "+s"(want)); want += (u64)phase;
        for (unsigned spins = 0; spins < (1u << 20); ++spins) {
            asm volatile("" ::: "memory");
            bool ok = true;
#pragma unroll
            for (int i = 0; i < PER_LANE; ++i) { const u32x2 f = __builtin_amdgcn_raw_buffer_load_b64(c.rs, pb * 128, OFF_FLAGS + (phase * 256 + 64 * i) * 128, 16);
                ok &= f[0] == (unsigned)want && f[1] == (unsigned)(want >> 32); }
            if (__all(ok)) break;
        }
    }
    BAR();
}

constexpr unsigned OOB = 0xF0000000u;

DI void qkv_store(const Ctx& c, const f32x16& acc, const f32x16& cs, float mu, float rstd, int kind, int pi, int l, int s, int j, int tb) {
    if (kind == 0) rstd *= 0.25f * 1.4426950408889634f;
    unsigned z[8];
#pragma unroll
    for (int i = 0; i < 8; ++i) { const f32x2 t = ((f32x2){acc[2 * i], acc[2 * i + 1]} - (f32x2){cs[2 * i], cs[2 * i + 1]} * mu) * rstd; z[i] = cvtpk(t[0], t[1]); }
    const int tbg = 2 * s + tb, hA = 4 * j + 2 * pi;
    const unsigned base = (kind == 0 ? OFF_Q : kind == 1 ? OFF_K : OFF_V) + l * ACT_B + (hA * 64 + tbg) * 1024;
    const unsigned vo = kind == 2 ? (c.lane & 31) * 32 + (c.lane >> 5) * 16 : c.l16;
    st16(c.rs, (u32x4){z[0], z[1], z[2], z[3]}, vo, base);
    st16(c.rs, (u32x4){z[4], z[5], z[6], z[7]}, vo, base + 64 * 1024);
}

DI void phase_first(Ctx& c0, const Params& p, int s, int j) {
    Ctx c = fresh(c0); const int lane = c.lane, w = c.wid, b = c.b;
    const rsrc_t rx = mkrsrc(p.x, 2048 * 512 * 2), rq = mkrsrc(p.wqkv, 512 * 1536 * 2);
    const unsigned vX = (lane >> 3) * 1024 + (((lane & 7) ^ ((((8 * w + (lane >> 3)) & 31) >> 1) & 7)) * 16);
    const unsigned vW = (lane >> 3) * 3072 + (((lane & 7) ^ (((lane >> 4) & 1) << 2)) * 16);
#define FISSUE(ch) do { const unsigned sb_ = c.lds0 + ((ch) & 3) * 32768; \
        dma_in(rx, vX, (64 * s + 8 * w) * 1024 + (ch) * 128, sb_ + w * 1024); \
        _Pragma("unroll") for (int i = 0; i < 3; ++i) { const int pi_ = 3 * w + i; \
            dma_in(rq, vW, (64 * (ch) + 8 * (pi_ & 7)) * 3072 + ((pi_ >> 3) * 512 + 64 * j) * 2, sb_ + 8192 + pi_ * 1024); } } while (0)
    u32x2 ep; asm volatile("buffer_load_dwordx2 %0, off, %1, 0 sc1" : "=v"(ep) : "s"(c.rs) : "memory");
    FISSUE(0); ORDER(); FISSUE(1); FISSUE(2); FISSUE(3);
    const int nb = w < 4 ? w : 4 + (w & 1), r = lane & 31, h2 = lane >> 5;
    unsigned xa[4];
#pragma unroll
    for (int k = 0; k < 4; ++k) xa[k] = (r >> 3) * 1024 + (r & 7) * 128 + (((2 * k + h2) ^ ((r >> 1) & 7)) * 16) + (w < 4 ? 0 : ((w >> 1) & 1) * 4096);
    unsigned wa;
    { const int g = lane >> 4, q = (lane & 15) >> 2, pp = lane & 3;
      wa = 8192 + (nb >> 1) * 8192 + (8 * (g >> 1) + q) * 128 + ((4 * (nb & 1) + 2 * (g & 1) + (pp >> 1)) ^ ((q >> 1) << 2)) * 16 + (pp & 1) * 8; }
    const bf16x8 ones = asbf((u32x4){0x3f803f80u, 0x3f803f80u, 0x3f803f80u, 0x3f803f80u});
    f32x16 acc0 = {}, acc1 = {}, cs = {};
    float sm = 0.f, sq = 0.f;
    u32x2 xg[4] = {};
    bf16x8 wfr[2][4]; u32x4 x0[2][4], x1[2][4], xr;
    const unsigned xo1 = w < 4 ? 4096 : 0;
#define FREAD1(ch, k) do { const unsigned sb_ = c.lds0 + ((ch) & 3) * 32768; \
        wfr[(ch) & 1][k] = cat(ldstr(sb_ + wa + (k) * 2048), ldstr(sb_ + wa + (k) * 2048 + 512)); x0[(ch) & 1][k] = lds16(sb_ + xa[k]); x1[(ch) & 1][k] = lds16(sb_ + xa[k] + xo1); \
        if ((k) == 0) xr = lds16(sb_ + w * 1024 + c.l16); \
        if ((k) == 3 && w < 4 && (ch) == j) {        \
            const unsigned t_ = 32 * (w >> 1) + r, a_ = sb_ + (t_ >> 3) * 1024 + (t_ & 7) * 128 + h2 * 8; \
            _Pragma("unroll") for (int m = 0; m < 4; ++m) xg[m] = *(const LAS u32x2*)(uintptr_t)(a_ + (((4 * (w & 1) + m) ^ ((r >> 1) & 7)) * 16)); } } while (0)
#define FISSUE1(ch, k) do { const unsigned sb_ = c.lds0 + ((ch) & 3) * 32768; \
        if ((k) == 0) dma_in(rx, vX, (64 * s + 8 * w) * 1024 + (ch) * 128, sb_ + w * 1024); \
        else { const int pi_ = 3 * w + (k) - 1; dma_in(rq, vW, (64 * (ch) + 8 * (pi_ & 7)) * 3072 + ((pi_ >> 3) * 512 + 64 * j) * 2, sb_ + 8192 + pi_ * 1024); } } while (0)
#define FSTEP(cm, rd, ci) do { \
        _Pragma("unroll") for (int k = 0; k < 4; ++k) { \
            if ((cm) >= 0) { acc0 = MFMA(wfr[(cm) & 1][k], asbf(x0[(cm) & 1][k]), acc0); \
                if (w < 4) acc1 = MFMA(wfr[(cm) & 1][k], asbf(x1[(cm) & 1][k]), acc1); \
                cs = MFMA(wfr[(cm) & 1][k], ones, cs); \
                asm volatile("" : "+v"(acc0), "+v"(acc1), "+v"(cs)); } \
            SBAR(); \
            if (rd) { FREAD1((cm) + 1, k); \
                if (k > 0) { const float lo = bflo(xr[k - 1]), hi = bfhi(xr[k - 1]); sm += lo + hi; sq += lo * lo + hi * hi; } \
                if (k == 3) { const float lo = bflo(xr[3]), hi = bfhi(xr[3]); sm += lo + hi; sq += lo * lo + hi * hi; } } \
            SBAR(); \
            if ((ci) >= 0) FISSUE1((ci) & 7, k); \
            SBAR(); } } while (0)
    rsrc_t rw = rq; unsigned ld = 1536, col0 = 0, ks0 = 4 * w, dst = OOB, csdst = OOB, src0 = OOB;
    if (b < 48) { const int jj = b / 6, i = b % 6; col0 = (i >> 1) * 512 + 64 * jj + 32 * (i & 1); dst = OFF_WQ + (b * 32 + ks0) * 1024; csdst = OFF_CSQ + b * 128; src0 = 0; }
    else if (b < 64) { rw = mkrsrc(p.wo, 512 * 512 * 2); ld = 512; col0 = 32 * (b - 48); dst = OFF_WO + ((b - 48) * 32 + ks0) * 1024; src0 = 0; }
    else if (b < 120) { rw = mkrsrc(p.w1, 512 * 1792 * 2); ld = 1792; col0 = 32 * (b - 64); dst = OFF_W1 + ((b - 64) * 32 + ks0) * 1024; csdst = OFF_CS1 + (b - 64) * 128; src0 = 0; }
    else if (b < 176) { rw = mkrsrc(p.w2, 1792 * 512 * 2); ld = 512; const int F = 32 * (b - 120) + 4 * w; col0 = 32 * (F / 112); ks0 = F % 112; dst = OFF_W2 + F * 1024; src0 = 0; }
    const unsigned stg = c.lds0 + w * 4096;
    WAITV_BAR(12);
    asm volatile("" : "+v"(ep));
    c.tag = c0.tag = (((u64)(unsigned)__builtin_amdgcn_readfirstlane(ep[1]) << 32) | (unsigned)__builtin_amdgcn_readfirstlane(ep[0])) + 1;
    FSTEP(-1, true, -1);
    WAITV_BAR(8); FSTEP(0, true, 4);
    WAITV_BAR(8); FSTEP(1, true, 5);
    WAITV_BAR(8); FSTEP(2, true, 6);
    WAITV_BAR(8); FSTEP(3, true, 7);
    WAITV_BAR(8);
    {
        const unsigned voff = ((lane >> 2) * ld + col0) * 2 + (lane & 3) * 16;
#pragma unroll
        for (int i = 0; i < 4; ++i) dma_in(rw, voff, src0 + (ks0 + i) * 16 * ld * 2, stg + i * 1024);
    }
    FSTEP(4, true, -1);
    WAITV_BAR(8); FSTEP(5, true, -1);
    WAITV_BAR(4); FSTEP(6, true, -1);
    sm = sum8(sm); sq = sum8(sq);
    if ((lane & 7) == 0) {
        const float mu = sm * (1.0f / 512.0f), var = fmaxf(sq * (1.0f / 512.0f) - mu * mu, 0.f);
        *(LAS f32x2*)(uintptr_t)(c.lds0 + LDS_XCH + (8 * w + (lane >> 3)) * 8) = (f32x2){mu, __builtin_amdgcn_rsqf(var + 1e-5f)};
    }
    SBAR();
    FSTEP(7, false, -1);
#undef FREAD1
#undef FISSUE1
#undef FISSUE
#undef FSTEP

    BAR();
    if (w < 4) {
        const unsigned o_ = OFF_XT + ((2 * s + (w >> 1)) * 32 + 4 * j + 2 * (w & 1)) * 1024;
        st16(c.rs, (u32x4){xg[0][0], xg[0][1], xg[1][0], xg[1][1]}, c.l16, o_); st16(c.rs, (u32x4){xg[2][0], xg[2][1], xg[3][0], xg[3][1]}, c.l16, o_ + 1024);
        const f32x2 t0 = *(const LAS f32x2*)(uintptr_t)(c.lds0 + LDS_XCH + r * 8), t1 = *(const LAS f32x2*)(uintptr_t)(c.lds0 + LDS_XCH + (32 + r) * 8);
        qkv_store(c, acc0, cs, t0[0], t0[1], w >> 1, w & 1, 0, s, j, 0);
        qkv_store(c, acc1, cs, t1[0], t1[1], w >> 1, w & 1, 0, s, j, 1);
    } else {
        const int tb = (w >> 1) & 1;
        const f32x2 t0 = *(const LAS f32x2*)(uintptr_t)(c.lds0 + LDS_XCH + (32 * tb + r) * 8);
        qkv_store(c, acc0, cs, t0[0], t0[1], 2, w & 1, 0, s, j, tb);
    }
    publish<0>(c, PH_QKV);
    {
        const int g = lane >> 4, hh = g >> 1, nh = g & 1, q = (lane & 15) >> 2, pp = lane & 3;
        const unsigned ta = stg + (4 * hh + q) * 64 + (16 * nh + 4 * pp) * 2;
        f32x16 cw = {};
#pragma unroll
        for (int i = 0; i < 4; ++i) {
            const bf16x8 f = cat(ldstr(ta + i * 1024), ldstr(ta + i * 1024 + 512));
            st16(c.rs, __builtin_bit_cast(u32x4, f), c.l16, dst + i * 1024);
            cw = MFMA(f, ones, cw);
        }
        if ((lane & 31) == 0) {
#pragma unroll
            for (int m = 0; m < 4; ++m) *(LAS f32x4*)(uintptr_t)(c.lds0 + LDS_XCH + 1024 + (w * 2 + (lane >> 5)) * 64 + m * 16) = (f32x4){cw[4 * m], cw[4 * m + 1], cw[4 * m + 2], cw[4 * m + 3]};
        }
        BAR();
        if (w == 0 && lane < 32) { float a = 0.f;
#pragma unroll
            for (int ww = 0; ww < 8; ++ww) a += *(const LAS float*)(uintptr_t)(c.lds0 + LDS_XCH + 1024 + ww * 128 + lane * 4);
            st4(c.rs, __float_as_uint(a), lane * 4, csdst); }
        publish<0>(c, PH_PREP);
    }
}

DI void issue_a512(const Ctx& c, int w, unsigned a_off  , unsigned st_off) {
    const unsigned tbo = (w >> 2) * 32768, n0 = 2 * (w & 3);
    dma<1>(c.rs, c.l16, st_off + w * 1024, c.lds0 + LDS_XCH + w * 1024);
    dma<2>(c.rs, c.l16, frag(a_off + tbo, n0), c.lds0 + tbo + n0 * 1024);
    ORDER();
#pragma unroll
    for (int ch = 1; ch < 4; ++ch) dma<2>(c.rs, c.l16, frag(a_off + tbo, n0 + 8 * ch), c.lds0 + tbo + (n0 + 8 * ch) * 1024);
    ORDER();
}
struct RowStat { f32x2 u[8]; };
DI void stat_read(const Ctx& c, int tb, RowStat& t) {
    const unsigned a = c.lds0 + LDS_XCH + tb * 4096 + (c.lane >> 5) * 2048 + (c.lane & 31) * 8;
#pragma unroll
    for (int i = 0; i < 8; ++i) t.u[i] = *(const LAS f32x2*)(uintptr_t)(a + i * 256);
}
DI void stat_finish(const RowStat& t, float& mu, float& rstd) {
    const f32x2 v = ((t.u[0] + t.u[1]) + (t.u[2] + t.u[3])) + ((t.u[4] + t.u[5]) + (t.u[6] + t.u[7]));
    const float sm = swap32_sum(v[0]), sq = swap32_sum(v[1]);
    mu = sm * (1.0f / 512.0f);
    rstd = __builtin_amdgcn_rsqf(fmaxf(sq * (1.0f / 512.0f) - mu * mu, 0.f) + 1e-5f);
}
#define KLOOP1(ab, wf, acc0, WAIT, AFTER) do { \
        u32x4 A0[4]; \
        WAIT(0); \
        A0[0] = lds16((ab)); A0[1] = lds16((ab) + 1024); A0[2] = lds16((ab) + 2048); SBAR(); \
        _Pragma("unroll") for (int ks = 0; ks < 32; ++ks) { \
            if (ks == 5) { WAIT(1); } \
            if (ks + 3 < 32) A0[(ks + 3) % 4] = lds16((ab) + (ks + 3) * 1024); \
            SBAR(); \
            acc0 = MFMA(asbf(wf[ks]), asbf(A0[ks % 4]), acc0); SBAR(); \
            AFTER(ks); SBAR(); \
        } } while (0)
#define NOTHING(x) do { } while (0)
DI void load_cs(const Ctx& c, unsigned off, f32x16& cs) {
#pragma unroll
    for (int m = 0; m < 4; ++m) { const f32x4 t = __builtin_bit_cast(f32x4, ld16(c.rs, (c.lane >> 5) * 64 + m * 16, off));
        cs[4 * m] = t[0]; cs[4 * m + 1] = t[1]; cs[4 * m + 2] = t[2]; cs[4 * m + 3] = t[3]; }
}

constexpr int QKV_P = 10, QKV_PRE = 16;
DI unsigned wq_off(int w, int j) { return OFF_WQ + (6 * j + (w < 4 ? w : 4 + (w & 1))) * 32 * 1024; }
DI void phase_qkv1(const Ctx& c0, int s, int j, u32x4 (&wf)[32]) {
    const Ctx c = fresh(c0); const int w = c.wid; OPAQUE_S(s); OPAQUE_S(j);
    const int nb = w < 4 ? w : 4 + (w & 1);
    const unsigned wo_ = wq_off(w, j);
    f32x16 cs; load_cs(c, OFF_CSQ + (6 * j + nb) * 128, cs);
    await<1>(c, PH_F2, blk_chain(s, c.lane & 7));
    issue_a512(c, w, OFF_XT + 2 * s * 32 * 1024, OFF_ST + ST_B + s * 8192);
    const unsigned ab = c.lds0 + c.l16;
#define QKV_WAIT(n) WAITV_BAR((n) == 0 ? 6 : 5)
#define QKV_LATE(ks) do { if (QKV_PRE + (ks) < 32) wf[(QKV_PRE + (ks)) & 31] = ld16(c.rs, c.l16, frag(wo_, QKV_PRE + (ks))); } while (0)
    {
        const int tb = w < 4 ? 0 : (w >> 1) & 1;
        f32x16 acc = {}; float mu, rs_; RowStat st;
#define QKV_AFTER(ks) do { QKV_LATE(ks); if ((ks) == 8) stat_read(c, tb, st); if ((ks) == 13) stat_finish(st, mu, rs_); } while (0)
        KLOOP1(ab + tb * 32768, wf, acc, QKV_WAIT, QKV_AFTER);
#undef QKV_AFTER
        qkv_store(c, acc, cs, mu, rs_, w < 4 ? w >> 1 : 2, w & 1, 1, s, j, tb);
    }
    if (w < 4) {
        f32x16 acc = {}; float mu, rs_; RowStat st;
#define QKV_AFTER(ks) do { if ((ks) == 2) stat_read(c, 1, st); if ((ks) == 7) stat_finish(st, mu, rs_); } while (0)
        KLOOP1(ab + 32768, wf, acc, NOTHING, QKV_AFTER);
#undef QKV_AFTER

        qkv_store(c, acc, cs, mu, rs_, w >> 1, w & 1, 1, s, j, 1);
    }
#undef QKV_WAIT
#undef QKV_LATE

    publish<0>(c, PH_QKV + PH_PER_LAYER);
}

constexpr int W1_PRE = 22, W1_MID = 10;
static_assert(W1_PRE + W1_MID == 32, "all of a wave's W1 fragments are requested before the Wo phase");
constexpr int ATT_PRIO_END = 16;
DI unsigned w1_off(int w, int j) { return w < 7 ? OFF_W1 + (7 * j + w) * 32 * 1024 : OOB; }
DI void phase_attn(const Ctx& c0, int l, int lp, int s, int j, u32x4 (&wf)[32], u32x4 (&wo)[8]) {
    OPAQUE_S(lp); OPAQUE_S(l); const Ctx c = fresh(c0); const int wid = c.wid, bb = c.b; OPAQUE_S(j); OPAQUE_S(s);
    const int lane = c.lane, h = (bb & 7) + 8 * ((bb >> 3) & 3), g = bb >> 5;
    const int qp = wid & 3, kh = wid >> 2;
    __builtin_amdgcn_s_waitcnt(waitcnt_imm(0, 15));
    for (int i = wid * 64 + lane; i < ONES_BYTES / 4; i += NT) *(LAS unsigned*)(uintptr_t)(c.lds0 + LDS_ONES + i * 4) = (i & 7) == 0 ? 0x00003f80u : 0u;
    await<1>(c, PH_QKV + lp, blk_chain(lane & 31, h >> 2));
    const unsigned kb = OFF_K + l * ACT_B + h * 65536, vb = OFF_V + l * ACT_B + h * 65536;
    u32x4 qrA, qrB;
    {
        const unsigned qo = OFF_Q + l * ACT_B + (h * 64 + 8 * g + 2 * qp) * 1024;
        asm volatile("s_nop 4\n\tbuffer_load_dwordx4 %0, %2, %3, %4 offen sc1\n\tbuffer_load_dwordx4 %1, %2, %3, %4 offen offset:1024 sc1"
                     : "=&v"(qrA), "=&v"(qrB) : "v"(c.l16), "s"(c.rs), "s"(qo) : "memory");
    }
    {
        const unsigned sk = c.lds0 + LDS_K + kh * 32768, sv = c.lds0 + LDS_V + kh * 32768;
        const unsigned tA = 8 * g + 4 * kh + qp;
        dma<1>(c.rs, c.l16, kb + tA * 1024, sk + qp * 1024);
        dma<1>(c.rs, c.l16, vb + tA * 1024, sv + qp * 1024);
    }
    asm volatile("s_waitcnt vmcnt(2)" : "+v"(qrA), "+v"(qrB) :: "memory");
    float mref = 0.f, mB = 0.f;
    const bf16x8 qA = asbf(qrA), qB = asbf(qrB);
    const int grp = lane >> 4, h2 = grp >> 1, isone = grp & 1, qq = (lane & 15) >> 2, pp = lane & 3;
    const int inrow = qq * 32 + (pp & 1) * 16 + (pp >> 1) * 8;
    const int vstep = isone ? 0 : ATT_UNR * 1024, vstep4 = isone ? 0 : 4096;
    for (int pass = 0;; ++pass) {
        unsigned kpa = c.lds0 + LDS_K + kh * 32768 + c.l16; asm volatile("" : "+v"(kpa));
        unsigned vpa = c.lds0 + (isone ? LDS_ONES + (h2 ? 0 : 128) + inrow : LDS_V + kh * 32768 + h2 * 128 + inrow); asm volatile("" : "+v"(vpa));
        f32x16 oA = {}, oB = {};
#define KRD(t) asbf(lds16(kpa + (t) * 1024))
#define VTR(off) ldstr(vpa + (off))
        WAITV_BAR(0);
        if (pass == 0) {
            const bf16x8 k0 = asbf(lds16(c.lds0 + LDS_K + c.l16)); const f32x16 zero = {};
            const f32x16 a = MFMA(k0, qA, zero), bq = MFMA(k0, qB, zero);
            float m0 = fmaxf(a[0], bq[0]), m1 = fmaxf(a[1], bq[1]);
#pragma unroll
            for (int i = 2; i < 16; i += 2) { m0 = fmaxf(fmaxf(m0, a[i]), bq[i]); m1 = fmaxf(fmaxf(m1, a[i + 1]), bq[i + 1]); }
            mref = swap32_max(fmaxf(m0, m1));
        }
        f32x16 nm;
#pragma unroll
        for (int i = 0; i < 16; ++i) nm[i] = -mref;
        asm volatile("" : "+v"(nm));
        bf16x8 kf = KRD(0);
        s16x4 va0 = VTR(0), va1 = VTR(256), vb0 = VTR(512), vb1 = VTR(768);
        f32x16 sA = MFMA(kf, qA, nm), sB;
        u32x4 p0, p1;
#define EX(cc, i) do { cc[i] = __builtin_amdgcn_exp2f(cc[i]); SBAR(); } while (0)
#define CV(p, jj, cc, i) do { p[jj] = cvtpk(cc[i], cc[i + 1]); SBAR(); } while (0)
#define STREAM(cur, ocur, prv, oprv, qprv, nmprv, tl, ISA, PEND) do { \
        SBAR(); \
        EX(cur, 0); EX(cur, 1); if (PEND) CV(p1, 2, prv, 12); EX(cur, 2); if (PEND) CV(p1, 3, prv, 14); EX(cur, 3); EX(cur, 4); \
        if (PEND) { oprv = MFMA(cat(vb0, vb1), asbf(p1), oprv); asm volatile("" : "+v"(oprv)); SBAR(); } \
        if (ISA && PEND) { vb0 = VTR((tl) * 1024 + 512); vb1 = VTR((tl) * 1024 + 768); SBAR(); } \
        CV(p0, 0, cur, 0); EX(cur, 5); EX(cur, 6); CV(p0, 1, cur, 2); EX(cur, 7); EX(cur, 8); \
        prv = MFMA(kf, qprv, nmprv); asm volatile("" : "+v"(prv)); SBAR(); \
        if (ISA) { kf = KRD((tl) + 1); SBAR(); } \
        CV(p0, 2, cur, 4); EX(cur, 9); EX(cur, 10); CV(p0, 3, cur, 6); EX(cur, 11); EX(cur, 12); \
        ocur = MFMA(cat(va0, va1), asbf(p0), ocur); asm volatile("" : "+v"(ocur)); SBAR(); \
        if (!(ISA)) { va0 = VTR(((tl) + 1) * 1024); va1 = VTR(((tl) + 1) * 1024 + 256); SBAR(); } \
        CV(p1, 0, cur, 8); EX(cur, 13); EX(cur, 14); CV(p1, 1, cur, 10); EX(cur, 15); \
    } while (0)
#define PAIR(tl, PEND) do { STREAM(sA, oA, sB, oB, qB, nm, tl, true, PEND); STREAM(sB, oB, sA, oA, qA, nm, tl, false, true); } while (0)
#define BUMP(n, vs) do { kpa += (n) * 1024; vpa += (vs); asm volatile("" : "+v"(kpa), "+v"(vpa)); } while (0)
        int wb_ = wid, bb_ = bb, lb_ = l; OPAQUE_S(wb_); OPAQUE_S(bb_); OPAQUE_S(lb_);
        const unsigned skb = c.lds0 + LDS_K + (wb_ >> 2) * 32768 + (4 + 2 * (wb_ & 3)) * 1024;
        const unsigned kbb = OFF_K + lb_ * ACT_B + ((bb_ & 7) + 8 * ((bb_ >> 3) & 3)) * 65536 + ((8 * (bb_ >> 5) + 8 + 28 * (wb_ >> 2) + 2 * (wb_ & 3)) & 63) * 1024;
#define STB(i) do { if ((i) < 4) dma<1>(c.rs, c.l16, kbb + ((i) & 1) * (OFF_V - OFF_K) + ((i) >> 1) * 1024, skb + ((i) & 1) * (LDS_V - LDS_K) + ((i) >> 1) * 1024); SBAR(); } while (0)
        STREAM(sA, oA, sB, oB, qB, nm, 0, true, false); STB(0);
        STREAM(sB, oB, sA, oA, qA, nm, 0, false, true); STB(1);
        STREAM(sA, oA, sB, oB, qB, nm, 1, true, true);  STB(2);
        STREAM(sB, oB, sA, oA, qA, nm, 1, false, true); STB(3);
        PAIR(2, true);
#undef STB
        WAITV_BAR(0);
        int wc_ = wid, bc_ = bb, lc_ = l; OPAQUE_S(wc_); OPAQUE_S(bc_); OPAQUE_S(lc_);
        const unsigned skc = c.lds0 + LDS_K + (wc_ >> 2) * 32768 + (12 + 5 * (wc_ & 3)) * 1024, ic0 = 8 * (bc_ >> 5) + 16 + 28 * (wc_ >> 2) + 5 * (wc_ & 3);
        const unsigned kbc = OFF_K + lc_ * ACT_B + ((bc_ & 7) + 8 * ((bc_ >> 3) & 3)) * 65536;
#define STC(i) do { if ((i) < 10) dma<1>(c.rs, c.l16, kbc + ((i) & 1) * (OFF_V - OFF_K) + ((ic0 + ((i) >> 1)) & 63) * 1024, skc + ((i) & 1) * (LDS_V - LDS_K) + ((i) >> 1) * 1024); SBAR(); } while (0)
        STREAM(sA, oA, sB, oB, qB, nm, 3, true, true);  STC(0);
        STREAM(sB, oB, sA, oA, qA, nm, 3, false, true); STC(1);
        BUMP(4, vstep4);
#pragma unroll
        for (int u = 0; u < ATT_UNR; ++u) {
            STREAM(sA, oA, sB, oB, qB, nm, u, true, true);  STC(2 + 2 * u);
            STREAM(sB, oB, sA, oA, qA, nm, u, false, true); STC(3 + 2 * u);
        }
#undef STC
        BUMP(ATT_UNR, vstep);
        WAITV_BAR(0);
        if (l == 0) await<4>(c, PH_PREP, lane);
        int w_ = wid, j_ = j, s_ = s; OPAQUE_S(w_); OPAQUE_S(j_); OPAQUE_S(s_);
        const unsigned o1 = OFF_WO + (2 * j_ + (w_ & 1)) * 32 * 1024, o2 = w1_off(w_, j_); const int q4_ = 4 * (w_ >> 1);
#define PF(i) do { if ((i) < 8) wo[(i) & 7] = ld16(c.rs, c.l16, frag(o1, 16 * (((i) & 7) >> 2) + ((i) & 3) + q4_)); \
        else if ((i) < 8 + W1_PRE) wf[((i) - 8) & 31] = ld16(c.rs, c.l16, frag(o2, (i) - 8)); SBAR(); } while (0)
        if (w_ >= 4) __builtin_amdgcn_s_setprio(1);
#pragma unroll
        for (int blk = 0; blk < 3; ++blk) {
#pragma unroll
            for (int u = 0; u < ATT_UNR; ++u) {
                if (ATT_UNR * blk + u == ATT_PRIO_END) __builtin_amdgcn_s_setprio(0);
                STREAM(sA, oA, sB, oB, qB, nm, u, true, true);  PF(2 * (ATT_UNR * blk + u));
                STREAM(sB, oB, sA, oA, qA, nm, u, false, true); PF(2 * (ATT_UNR * blk + u) + 1);
            }
            BUMP(ATT_UNR, vstep);
        }
#undef PF
        CV(p1, 2, sB, 12); CV(p1, 3, sB, 14);
        oB = MFMA(cat(vb0, vb1), asbf(p1), oB);
        {
            const f32x16 give = kh ? oA : oB;
            const unsigned xb = c.lds0 + LDS_XCH + wid * 2304;
            *(LAS f32x4*)(uintptr_t)(xb + c.l16) = (f32x4){give[0], give[1], give[2], give[3]};
            *(LAS f32x4*)(uintptr_t)(xb + 1024 + c.l16) = (f32x4){give[4], give[5], give[6], give[7]};
            *(LAS float*)(uintptr_t)(xb + 2048 + lane * 4) = give[8];
            if (wid == 0 && lane == 0) *(LAS unsigned*)(uintptr_t)(c.lds0 + LDS_FLAG) = 0u;
        }
        BAR();
        f32x16 o = kh ? oB : oA;
        {
            const unsigned xb = c.lds0 + LDS_XCH + (wid ^ 4) * 2304;
            const f32x4 u0 = ldsf4(xb + c.l16), u1 = ldsf4(xb + 1024 + c.l16);
            const float ul = *(const LAS float*)(uintptr_t)(xb + 2048 + lane * 4);
#pragma unroll
            for (int i = 0; i < 4; ++i) { o[i] += u0[i]; o[4 + i] += u1[i]; }
            o[8] = swap32_lo(o[8] + ul);
        }
        const float rl = __builtin_amdgcn_rcpf(o[8]);
        int w2_ = wid, b2_ = bb, l2_ = l; OPAQUE_S(w2_); OPAQUE_S(b2_); OPAQUE_S(l2_);
        const unsigned o_off = OFF_O + l2_ * ACT_B + ((8 * (b2_ >> 5) + 2 * (w2_ & 3) + (w2_ >> 2)) * 32 + (b2_ & 7) + 8 * ((b2_ >> 3) & 3)) * 1024;
        st16(c.rs, (u32x4){cvtpk(o[0] * rl, o[1] * rl), cvtpk(o[2] * rl, o[3] * rl), cvtpk(o[4] * rl, o[5] * rl), cvtpk(o[6] * rl, o[7] * rl)}, c.l16,
             pass == 0 || pass == 1 + (w2_ >> 2) ? o_off : OOB);
        const float omax = fmaxf(fmaxf(fmaxf(fabsf(o[0]), fabsf(o[1])), fmaxf(fabsf(o[2]), fabsf(o[3]))), fmaxf(fmaxf(fabsf(o[4]), fabsf(o[5])), fmaxf(fabsf(o[6]), fabsf(o[7]))));
        const bool bad = !(o[8] >= 8.673617379884035e-19f   && o[8] <= 1.152921504606847e18f   && omax <= 3.0e38f);
        if (pass == 0 && __any(bad) && lane == 0) *(LAS unsigned*)(uintptr_t)(c.lds0 + LDS_FLAG) = 1u;
        WAITV_BAR(0);
        if (pass == 2 || (pass == 0 && __builtin_amdgcn_readfirstlane(*(const LAS unsigned*)(uintptr_t)(c.lds0 + LDS_FLAG)) == 0u)) break;
        if (pass == 0) {
            float mA = -3.0e38f; mB = -3.0e38f;
            const f32x16 zero = {};
            for (int t = 0; t < 64; ++t) {
                const bf16x8 kk = asbf(lds16(c.lds0 + LDS_K + t * 1024 + c.l16));
                const f32x16 a = MFMA(kk, qA, zero), bq = MFMA(kk, qB, zero);
#pragma unroll
                for (int i = 0; i < 16; ++i) { mA = fmaxf(mA, a[i]); mB = fmaxf(mB, bq[i]); }
            }
            mref = swap32_max(mA); mB = swap32_max(mB);
        } else mref = mB;
        BAR();
#undef KRD
#undef VTR
#undef EX
#undef CV
#undef STREAM
#undef PAIR
#undef BUMP
    }
    if (c.wid == 0 && c.lane == 0) { u64 t = c.tag; asm volatile("" : "+s"(t)); t += (u64)(PH_ATT + lp);
        st8(c.rs, (u32x2){(unsigned)t, (unsigned)(t >> 32)}, 0, OFF_FLAGS + ((PH_ATT + lp) * 256 + c.b) * 128); }
    asm volatile("" ::: "memory");
    {
        int w_ = wid, j_ = j, s_ = s; OPAQUE_S(w_); OPAQUE_S(j_); OPAQUE_S(s_);
        const unsigned o2 = w1_off(w_, j_);
#pragma unroll
        for (int i = W1_PRE; i < W1_PRE + W1_MID; ++i) wf[i] = ld16(c.rs, c.l16, frag(o2, i));
    }
}

DI void res_finish(const Ctx& c, const Params& p, int w, int s, int j, bool last, const f32x16& v, unsigned x_off, unsigned st_off, unsigned (&xres)[8]) {
    const int lane = c.lane, tb = w >> 1, nb = w & 1;
    f32x2 y[8];
#pragma unroll
    for (int m = 0; m < 8; ++m) { y[m] = (f32x2){bflo(xres[m]), bfhi(xres[m])} + (f32x2){v[2 * m], v[2 * m + 1]}; xres[m] = cvtpk(y[m][0], y[m][1]); }
    if (last) {
        const rsrc_t ro = mkrsrc(p.out, 2048 * 512 * 2);
        const unsigned vo = (64 * s + 32 * tb + (lane & 31)) * 1024 + (64 * j + 32 * nb) * 2 + (lane >> 5) * 16;
#pragma unroll
        for (int k = 0; k < 2; ++k) {
            const auto a = __builtin_amdgcn_permlane32_swap(xres[4 * k], xres[4 * k + 2], false, false);
            const auto b = __builtin_amdgcn_permlane32_swap(xres[4 * k + 1], xres[4 * k + 3], false, false);
            __builtin_amdgcn_raw_buffer_store_b128((u32x4){a[0], b[0], a[1], b[1]}, ro, vo + 32 * k, 0, 0);
        }
        return;
    }
    const unsigned base = x_off + ((2 * s + tb) * 32 + 4 * j + 2 * nb) * 1024;
    st16(c.rs, (u32x4){xres[0], xres[1], xres[2], xres[3]}, c.l16, base);
    st16(c.rs, (u32x4){xres[4], xres[5], xres[6], xres[7]}, c.l16, base + 1024);
    f32x2 s2 = (y[0] + y[1]) + (y[2] + y[3]), q2 = y[0] * y[0];
    s2 += (y[4] + y[5]) + (y[6] + y[7]);
#pragma unroll
    for (int m = 1; m < 8; ++m) q2 += y[m] * y[m];
    const float sm = swap32_sum(s2[0] + s2[1]), sq = swap32_sum(q2[0] + q2[1]);
    if (lane < 32) st8(c.rs, (u32x2){__float_as_uint(sm), __float_as_uint(sq)}, lane * 8, st_off + (2 * s + tb) * 4096 + (2 * j + nb) * 256);
}
DI void put16(unsigned a, const f32x16& v) {
#pragma unroll
    for (int m = 0; m < 4; ++m) *(LAS f32x4*)(uintptr_t)(a + m * 1024) = (f32x4){v[4 * m], v[4 * m + 1], v[4 * m + 2], v[4 * m + 3]};
}
DI void add16x3(unsigned a, f32x16& v) {
    f32x4 t[12];
#pragma unroll
    for (int m = 0; m < 12; ++m) t[m] = ldsf4(a + m * 1024);
#pragma unroll
    for (int m = 0; m < 4; ++m) { const f32x4 u = (t[m] + t[4 + m]) + t[8 + m]; v[4 * m] += u[0]; v[4 * m + 1] += u[1]; v[4 * m + 2] += u[2]; v[4 * m + 3] += u[3]; }
}

DI void parts_out(unsigned base, int nb, int q, int tbA, const f32x16& acc0, const f32x16& acc1) {
    const int sl = q > 0 ? q - 1 : 0;
    put16(base + ((nb + 2 * (1 - tbA)) * 3 + sl) * 4096, acc1);
    if (q >= 2) put16(base + (nb * 3 + sl) * 4096, acc0);
}
DI void phase_wo(const Ctx& c0, const Params& p, int l, int lp, int s, int j, unsigned (&xres)[8], u32x4 (&wo)[8]) {
    OPAQUE_S(lp); OPAQUE_S(l); const Ctx c = fresh(c0); const int w = c.wid; OPAQUE_S(s); OPAQUE_S(j);
    const int nb = w & 1, q = w >> 1, tbA = q == 1 ? 1 : 0;
    if (w < 4) {
        const unsigned o = OFF_XT + ((2 * s + (w >> 1)) * 32 + 4 * j + 2 * nb) * 1024;
        const u32x4 a = ld16(c.rs, c.l16, o), b = ld16(c.rs, c.l16, o + 1024);
#pragma unroll
        for (int m = 0; m < 4; ++m) { xres[m] = a[m]; xres[4 + m] = b[m]; }
    }
    await<1>(c, PH_ATT + lp, blk_attn(c.lane & 31, s >> 2));
    const unsigned a_off = OFF_O + l * ACT_B + 2 * s * 32 * 1024;

#define ISSUE(n) do { const unsigned o_ = 16 * ((n) >> 1) + 4 * (w & 3) + 2 * ((n) & 1); \
        dma<2>(c.rs, c.l16, frag(a_off + (w >> 2) * 32768, o_), c.lds0 + (w >> 2) * 32768 + o_ * 1024); } while (0)
    ISSUE(0); ORDER(); ISSUE(1); ISSUE(2); ISSUE(3); ORDER();
#undef ISSUE
    f32x16 acc0 = {}, acc1 = {};
    const unsigned ab0 = c.lds0 + c.l16 + q * 4096 + tbA * 32768, ab1 = c.lds0 + c.l16 + q * 4096 + (1 - tbA) * 32768;
#define COMPUTE(n) do { const unsigned o_ = (16 * ((n) >> 1) + 2 * ((n) & 1)) * 1024; u32x4 A0[2], A1[2]; \
        _Pragma("unroll") for (int i = 0; i < 2; ++i) { A0[i] = lds16(ab0 + o_ + i * 1024); A1[i] = lds16(ab1 + o_ + i * 1024); } SBAR(); \
        _Pragma("unroll") for (int i = 0; i < 2; ++i) { \
            acc0 = MFMA(asbf(wo[2 * (n) + i]), asbf(A0[i]), acc0); acc1 = MFMA(asbf(wo[2 * (n) + i]), asbf(A1[i]), acc1); SBAR(); } } while (0)
    WAITV_BAR(6); COMPUTE(0);
    WAITV_BAR(0); COMPUTE(1); COMPUTE(2); COMPUTE(3);
#undef COMPUTE

    parts_out(c.lds0 + 65536 + c.l16, nb, q, tbA, acc0, acc1);
    BAR();
    if (w < 4) {
        const unsigned xa = c.lds0 + 65536 + w * 3 * 4096 + c.l16;
        add16x3(xa, acc0);
        res_finish(c, p, w, s, j, false, acc0, OFF_X1 + l * ACT_B, OFF_ST + (2 * l) * ST_B, xres);
    }
    publish<0>(c, PH_WO + lp);


}

constexpr int W2_MID = 12;
DI unsigned w2_off(int w, int j) { return OFF_W2 + ((2 * j + (w & 1)) * 112 + 4 * (w >> 1)) * 1024; }
DI void phase_f1(const Ctx& c0, int l, int lp, int s, int j, u32x4 (&wf)[32]) {
    OPAQUE_S(lp); OPAQUE_S(l); const Ctx c = fresh(c0); const int w = c.wid; OPAQUE_S(s); OPAQUE_S(j);
    const int nb = 7 * j + w;
    f32x16 cs; load_cs(c, w < 7 ? OFF_CS1 + nb * 128 : OOB, cs);
    await<1>(c, PH_WO + lp, blk_chain(s, c.lane & 7));


    issue_a512(c, w, OFF_X1 + l * ACT_B + 2 * s * 32 * 1024, OFF_ST + (2 * l) * ST_B + s * 8192);
    const unsigned ab = c.lds0 + c.l16, nxt = w2_off(w, j);
#define W2FRAG(i) ld16(c.rs, c.l16, nxt + (16 * ((i) >> 2) + ((i) & 3)) * 1024)
#define F1_WAIT(n) WAITV_BAR((n) == 0 ? 6 : 0)
#define F1_OUT(tb) do { float z[16]; \
        _Pragma("unroll") for (int i = 0; i < 8; ++i) { const f32x2 t = ((f32x2){acc[2 * i], acc[2 * i + 1]} - (f32x2){cs[2 * i], cs[2 * i + 1]} * mu) * rs_; z[2 * i] = t[0]; z[2 * i + 1] = t[1]; } \
        const unsigned base = w < 7 ? OFF_H + l * H_B + ((2 * s + (tb)) * 112 + 2 * nb) * 1024 : OOB; \
        st16(c.rs, (u32x4){relupk(z[0], z[1]), relupk(z[2], z[3]), relupk(z[4], z[5]), relupk(z[6], z[7])}, c.l16, base); \
        st16(c.rs, (u32x4){relupk(z[8], z[9]), relupk(z[10], z[11]), relupk(z[12], z[13]), relupk(z[14], z[15])}, c.l16, base + 1024); } while (0)
    {
        f32x16 acc = {}; float mu, rs_; RowStat st;
#define F1_AFTER(ks) do { if ((ks) == 8) stat_read(c, 0, st); if ((ks) == 13) stat_finish(st, mu, rs_); } while (0)
        KLOOP1(ab, wf, acc, F1_WAIT, F1_AFTER);
#undef F1_AFTER
        F1_OUT(0);
    }
    {
        f32x16 acc = {}; float mu, rs_; RowStat st;
#define F1_AFTER(ks) do { if ((ks) < W2_MID) wf[ks] = W2FRAG(ks); if ((ks) == 22) stat_read(c, 1, st); if ((ks) == 27) stat_finish(st, mu, rs_); } while (0)
        KLOOP1(ab + 32768, wf, acc, NOTHING, F1_AFTER);
#undef F1_AFTER


        F1_OUT(1);
    }
#undef F1_WAIT
#undef F1_OUT

    publish<0>(c, PH_F1 + lp);

#pragma unroll
    for (int i = W2_MID; i < 28; ++i) wf[i] = W2FRAG(i);
#undef W2FRAG

}

DI void phase_f2(const Ctx& c0, const Params& p, int l, int lp, int s, int j, unsigned (&xres)[8], u32x4 (&wf)[32]) {
    OPAQUE_S(lp); OPAQUE_S(l); const Ctx c = fresh(c0); const int w = c.wid; OPAQUE_S(s); OPAQUE_S(j);
    const int nb = w & 1, q = w >> 1, tbA = q == 1 ? 1 : 0;
    const bool last = l != 0;
    await<1>(c, PH_F1 + lp, blk_chain(s, c.lane & 7));
    const unsigned a_off = OFF_H + l * H_B + 2 * s * 112 * 1024;
#define ISSUE(ch) dma<4>(c.rs, c.l16, a_off + ((w >> 2) * 112 + 16 * (ch) + 4 * (w & 3)) * 1024, c.lds0 + ((ch) & 3) * 32768 + (w >> 2) * 16384 + 4 * (w & 3) * 1024)
    f32x16 acc0 = {}, acc1 = {};
    const unsigned ab0 = c.lds0 + c.l16 + q * 4096 + tbA * 16384, ab1 = c.lds0 + c.l16 + q * 4096 + (1 - tbA) * 16384;
#define COMPUTE(ch) do { const unsigned o_ = ((ch) & 3) * 32768; u32x4 A0[4], A1[4]; \
        _Pragma("unroll") for (int i = 0; i < 2; ++i) { A0[i] = lds16(ab0 + o_ + i * 1024); A1[i] = lds16(ab1 + o_ + i * 1024); } SBAR(); \
        _Pragma("unroll") for (int i = 0; i < 4; ++i) { \
            if (i < 2) { A0[i + 2] = lds16(ab0 + o_ + (i + 2) * 1024); A1[i + 2] = lds16(ab1 + o_ + (i + 2) * 1024); } SBAR(); \
            acc0 = MFMA(asbf(wf[4 * (ch) + i]), asbf(A0[i]), acc0); acc1 = MFMA(asbf(wf[4 * (ch) + i]), asbf(A1[i]), acc1); SBAR(); } } while (0)
    const unsigned nxt = wq_off(w, j);
    constexpr int P = QKV_P;
    ISSUE(0); ORDER(); ISSUE(1); ORDER(); ISSUE(2); ISSUE(3); ORDER();
    WAITV_BAR(12); COMPUTE(0);
    WAITV_BAR(8); ISSUE(4); COMPUTE(1);
    WAITV_BAR(8); ISSUE(5); COMPUTE(2);
    WAITV_BAR(8); ISSUE(6); COMPUTE(3);
    WAITV_BAR(8); COMPUTE(4);
    WAITV_BAR(4); COMPUTE(5);
    WAITV_BAR(0); COMPUTE(6);
#undef ISSUE
#undef COMPUTE

    parts_out(c.lds0 + c.l16, nb, q, tbA, acc0, acc1);
    BAR();
    if (w < 4) {
        const unsigned xa = c.lds0 + w * 3 * 4096 + c.l16;
        add16x3(xa, acc0);
        res_finish(c, p, w, s, j, last, acc0, OFF_XT, OFF_ST + ST_B, xres);
    }
    if (last) { __builtin_amdgcn_s_waitcnt(waitcnt_imm(0, 15)); return; }
    asm volatile("" ::: "memory");
    if (w >= 4) {
#pragma unroll
        for (int i = 0; i < P; ++i) wf[i] = ld16(c.rs, c.l16, frag(nxt, i));
    }
    asm volatile("" ::: "memory");
    if (w < 4) __builtin_amdgcn_s_waitcnt(waitcnt_imm(0, 15));
    publish<63>(c, PH_F2);
    if (w < 4) {
#pragma unroll
        for (int i = 0; i < P; ++i) wf[i] = ld16(c.rs, c.l16, frag(nxt, i));
    }
#pragma unroll
    for (int i = P; i < QKV_PRE; ++i) wf[i] = ld16(c.rs, c.l16, frag(nxt, i));
}

__global__ void __launch_bounds__(NT, 2) fused_layers_kernel(Params p) {
    extern __shared__ __attribute__((aligned(16))) char lds[];
    Ctx c;
    c.rs = mkrsrc(p.ws, WS_BYTES);
    c.lds0 = (unsigned)(uintptr_t)(LAS char*)lds;
    c.lane = threadIdx.x & 63; c.wid = __builtin_amdgcn_readfirstlane(threadIdx.x >> 6); c.l16 = c.lane * 16; c.b = blockIdx.x;
    c.tag = 0;
    const int x = c.b & 7, r = c.b >> 3, s = 4 * x + (r >> 3), j = r & 7;
    {
        u64 pc; asm volatile("s_getpc_b64 %0" : "=s"(pc));
        if (c.wid == 7) warm4k(mkrsrc((const void*)(pc & ~(u64)63), 36 * 1024), r < 9 ? r * 4096 + c.lane * 64 : OOB, 0, c.lds0 + LDS_DUMMY);
    }
    unsigned xres[8];
    u32x4 wf[32], wo[8];
    phase_first(c, p, s, j);

#pragma unroll 1
    for (int l_ = 0;; ++l_) {
        int l = __builtin_amdgcn_readfirstlane(l_); OPAQUE_S(l);
        int lp = PH_PER_LAYER * l; OPAQUE_S(lp);
        phase_attn(c, l, lp, s, j, wf, wo);
        phase_wo(c, p, l, lp, s, j, xres, wo);
        phase_f1(c, l, lp, s, j, wf);
        phase_f2(c, p, l, lp, s, j, xres, wf);
        if (l != 0) break;
        phase_qkv1(c, s, j, wf);
    }
    const Ctx ce = fresh(c);
    if (ce.b == 0 && ce.wid == 0 && ce.lane == 0) { const u64 t = ce.tag + 15; st8(ce.rs, (u32x2){(unsigned)t, (unsigned)(t >> 32)}, 0, OFF_EPOCH); }
}

}

extern "C" void kernel_launch(void* const* d_in, const int* in_sizes, int n_in, void* d_out, int out_size, void* d_ws, size_t ws_size, hipStream_t stream) {
    (void)in_sizes; (void)n_in; (void)out_size; (void)ws_size;
    Params p;
    p.x = d_in[0]; p.wqkv = d_in[1]; p.wo = d_in[2]; p.w1 = d_in[4]; p.w2 = d_in[5]; p.out = d_out; p.ws = d_ws;
    (void)hipFuncSetAttribute((const void*)fused_layers_kernel, hipFuncAttributeMaxDynamicSharedMemorySize, LDS_TOTAL);
    hipLaunchKernelGGL(fused_layers_kernel, dim3(256), dim3(NT), LDS_TOTAL, stream, p);
}
```
